```python
import torch

def _softmax(x, axis=-1):
    return torch.softmax(x, dim=axis)
def _gelu(x):
    return torch.nn.functional.gelu(x, approximate="tanh")
def _silu(x):
    return torch.nn.functional.silu(x)
def _sigmoid(x):
    return torch.sigmoid(x)
def _layernorm(x, axis=-1, eps=1e-5):
    m = x.mean(dim=axis, keepdim=True)
    v = x.var(dim=axis, keepdim=True, unbiased=False)
    return (x - m) / torch.sqrt(v + eps)
def _scatter_add(tgt, idx, src, axis=0):
    return tgt.clone().index_add_(axis, idx, src)
def _segment_sum(src, seg, n):
    out = torch.zeros((n,) + src.shape[1:], dtype=src.dtype)
    return out.index_add_(0, seg, src)
def _pad(x, pw):
    flat = tuple(v for pair in reversed(pw) for v in pair)
    return torch.nn.functional.pad(x, flat)

kernel_name = 'standalone__gqa_block_cached__near_ridge'

def setup_inputs():

    _w = (lambda a: (lambda t: t.to(torch.bfloat16) if t.is_floating_point() else t)(torch.as_tensor(a)))
    B, Sq, D = 8, 64, 576
    Hq, Hkv, Sk, Dh = 32, 16, 512, 128
    assert Hq % Hkv == 0, (Hq, Hkv)
    return {
        "x": _w((lambda *s: torch.randn(*s, dtype=torch.bfloat16) * 0.8)(B, Sq, D)),
        "k_cache": _w((lambda *s: torch.randn(*s, dtype=torch.bfloat16) * 0.8)(B, Hkv, Sk, Dh)),
        "v_cache": _w((lambda *s: torch.randn(*s, dtype=torch.bfloat16) * 0.05)(B, Hkv, Sk, Dh)),
        "wq": _w((lambda *s: torch.randn(*s, dtype=torch.bfloat16) * 0.8)(D, Hq * Dh) * (D ** -0.5)),
        "wo": _w((lambda *s: torch.randn(*s, dtype=torch.bfloat16) * 0.05)(Hq * Dh, D) * ((Hq * Dh) ** -0.5)),
    }

def reference(x, k_cache, v_cache, wq, wo):
    B, Sq, D = x.shape
    Hq, Dh = 32, 128
    Hkv = k_cache.shape[1]
    rep = Hq // Hkv
    q = (lambda x, *d: x.permute(*d))((x @ wq).reshape(B, Sq, Hq, Dh), 0, 2, 1, 3)
    k = (lambda x, n, axis: torch.repeat_interleave(x, n, dim=axis))(k_cache, rep, axis=1)
    v = (lambda x, n, axis: torch.repeat_interleave(x, n, dim=axis))(v_cache, rep, axis=1)
    scores = torch.einsum("bhqd,bhkd->bhqk", q, k) * (Dh ** -0.5)
    p = _softmax(scores, axis=-1)
    o = torch.einsum("bhqk,bhkd->bhqd", p, v)
    return (lambda x, *d: x.permute(*d))(o, 0, 2, 1, 3).reshape(B, Sq, Hq * Dh) @ wo
```

```cpp
#include <hip/hip_runtime.h>
#include <hip/hip_bf16.h>
#include <stdint.h>

typedef unsigned v4u __attribute__((ext_vector_type(4)));
typedef unsigned v2u __attribute__((ext_vector_type(2)));
typedef float f32x4 __attribute__((ext_vector_type(4)));
typedef short bf16x8 __attribute__((ext_vector_type(8)));
typedef __bf16 bf2 __attribute__((ext_vector_type(2)));
typedef float f32x2 __attribute__((ext_vector_type(2)));
#define LAS __attribute__((address_space(3)))

constexpr int B_ = 8, SQ = 64, D_ = 576, HQ = 32, HKV = 16, SK = 512, DH = 128;
constexpr int RSRC_W3 = 0x00020000;

__device__ __forceinline__ f32x4 mfma(v4u a, v4u b, f32x4 c) {
  return __builtin_amdgcn_mfma_f32_16x16x32_bf16(__builtin_bit_cast(bf16x8, a), __builtin_bit_cast(bf16x8, b), c, 0, 0, 0);
}
__device__ __forceinline__ unsigned pk2(float lo, float hi) {
  f32x2 v = {lo, hi};
  bf2 r = __builtin_convertvector(v, bf2);
  return __builtin_bit_cast(unsigned, r);
}
__device__ __forceinline__ v4u ld128(__amdgpu_buffer_rsrc_t rs, int voff, int soff) {
  return __builtin_amdgcn_raw_buffer_load_b128(rs, voff, soff, 0);
}
#define SBAR() __builtin_amdgcn_sched_barrier(0)

__device__ __forceinline__ v4u tr_col(const v4u (&r)[8], int e) {
  const unsigned sel = (e & 1) ? 0x07060302u : 0x05040100u;
  v4u o;
  o.x = __builtin_amdgcn_perm(r[1][e >> 1], r[0][e >> 1], sel);
  o.y = __builtin_amdgcn_perm(r[3][e >> 1], r[2][e >> 1], sel);
  o.z = __builtin_amdgcn_perm(r[5][e >> 1], r[4][e >> 1], sel);
  o.w = __builtin_amdgcn_perm(r[7][e >> 1], r[6][e >> 1], sel);
  return o;
}

#define STAMP_AT(i)

constexpr int LDS1 = 96 * 1024 + 16 * 1024 + 4096;

__global__ __launch_bounds__(256, 1) void k_attn(const uint16_t* __restrict__ x, const uint16_t* __restrict__ kc,
                                                 const uint16_t* __restrict__ vc, const uint16_t* __restrict__ wq,
                                                 uint16_t* __restrict__ ows) {
  constexpr int STAMP_BASE = 0;
  STAMP_AT(0);
  extern __shared__ __attribute__((aligned(16))) char smem[];
  LAS char* L = (LAS char*)smem;
  const int bid = blockIdx.x;
  const int h = 4 * (bid & 7) + ((bid >> 3) & 3);
  const int b = bid >> 5;
  const int kvh = h >> 1;
  const int w = __builtin_amdgcn_readfirstlane(threadIdx.x >> 6);
  const int lane = threadIdx.x & 63, c = lane & 15, g = lane >> 4;

  const __amdgpu_buffer_rsrc_t rx = __builtin_amdgcn_make_buffer_rsrc((void*)(x + (size_t)b * SQ * D_), 0, SQ * D_ * 2, RSRC_W3);
  const __amdgpu_buffer_rsrc_t rw = __builtin_amdgcn_make_buffer_rsrc((void*)(wq + h * DH), 0, (D_ * HQ * DH - h * DH) * 2, RSRC_W3);
  const __amdgpu_buffer_rsrc_t rk = __builtin_amdgcn_make_buffer_rsrc((void*)(kc + ((size_t)(b * HKV + kvh) * SK + 128 * w) * DH), 0, 128 * DH * 2, RSRC_W3);
  const __amdgpu_buffer_rsrc_t rv = __builtin_amdgcn_make_buffer_rsrc((void*)(vc + ((size_t)(b * HKV + kvh) * SK + 128 * w) * DH), 0, 128 * DH * 2, RSRC_W3);

  const int vo_w = g * 8 * 8192 + c * 16;
  const int vo_x = c * 1152 + g * 16;
  const int vo_k = c * 256 + g * 16;
  const int vo_v = g * 1024 + c * 16;

  v4u wr[3][8], xf[3][4];
#define LOADQ(i, slot) do { const int s_ = w + 4 * (i); const int sx_ = s_ < 18 ? s_ : 0; \
    _Pragma("unroll") for (int j = 0; j < 8; ++j) wr[slot][j] = ld128(rw, vo_w, (s_ * 32 + j) * 8192); \
    _Pragma("unroll") for (int t = 0; t < 4; ++t) xf[slot][t] = ld128(rx, vo_x, t * 16 * 1152 + sx_ * 64); SBAR(); } while (0)
#define QSTEP(slot) do { _Pragma("unroll") for (int e = 0; e < 8; ++e) { const v4u a = tr_col(wr[slot], e); \
    _Pragma("unroll") for (int t = 0; t < 4; ++t) acc[e][t] = mfma(a, xf[slot][t], acc[e][t]); } SBAR(); } while (0)
  LOADQ(0, 0); LOADQ(1, 1); LOADQ(2, 2);
  v4u kf[8][4];
#pragma unroll
  for (int kt = 0; kt < 4; ++kt)
#pragma unroll
    for (int s = 0; s < 4; ++s) kf[kt][s] = ld128(rk, vo_k + s * 64, kt * 4096);
  SBAR();

  f32x4 acc[8][4];
#pragma unroll
  for (int e = 0; e < 8; ++e)
#pragma unroll
    for (int t = 0; t < 4; ++t) acc[e][t] = f32x4{0, 0, 0, 0};
  QSTEP(0); LOADQ(3, 0);
  QSTEP(1); LOADQ(4, 1);
#pragma unroll
  for (int kt = 4; kt < 8; ++kt)
#pragma unroll
    for (int s = 0; s < 4; ++s) kf[kt][s] = ld128(rk, vo_k + s * 64, kt * 4096);
  SBAR();
  QSTEP(2); QSTEP(0); QSTEP(1);
  v4u vr[4][8];
#pragma unroll
  for (int u = 0; u < 4; ++u)
#pragma unroll
    for (int j = 0; j < 8; ++j) vr[u][j] = ld128(rv, vo_v + (j & 3) * 256, u * 8192 + (j >> 2) * 4096);
  SBAR();
  STAMP_AT(1);

  LAS f32x4* red = (LAS f32x4*)L;
#pragma unroll
  for (int t = 0; t < 4; ++t) {
    if (t != w) {
      const int slot = (w - t - 1) & 3;
#pragma unroll
      for (int e = 0; e < 8; ++e) red[((t * 3 + slot) * 8 + e) * 64 + lane] = acc[e][t];
    }
  }
  __syncthreads();
  f32x4 own[8];
#pragma unroll
  for (int e = 0; e < 8; ++e) own[e] = w == 0 ? acc[e][0] : w == 1 ? acc[e][1] : w == 2 ? acc[e][2] : acc[e][3];
#pragma unroll
  for (int sl = 0; sl < 3; ++sl)
#pragma unroll
    for (int e = 0; e < 8; ++e) own[e] += red[((w * 3 + sl) * 8 + e) * 64 + lane];
  LAS v4u* qf = (LAS v4u*)(L + 96 * 1024);
#pragma unroll
  for (int i = 0; i < 4; ++i) {
    v4u p = {pk2(own[0][i], own[1][i]), pk2(own[2][i], own[3][i]), pk2(own[4][i], own[5][i]), pk2(own[6][i], own[7][i])};
    qf[(w * 4 + g) * 64 + i * 16 + c] = p;
  }
  __syncthreads();
  v4u q[4][4];
#pragma unroll
  for (int t = 0; t < 4; ++t)
#pragma unroll
    for (int s = 0; s < 4; ++s) q[t][s] = qf[(t * 4 + s) * 64 + lane];
  STAMP_AT(2);

  f32x4 sc[8][4];
#pragma unroll
  for (int kt = 0; kt < 8; ++kt)
#pragma unroll
    for (int t = 0; t < 4; ++t) {
      f32x4 a = {0, 0, 0, 0};
#pragma unroll
      for (int s = 0; s < 4; ++s) a = mfma(kf[kt][s], q[t][s], a);
      sc[kt][t] = a;
    }
  constexpr float CS = 0.088388347648318447f * 1.4426950408889634f;
  float mx[4], ls[4];
  v4u pf[4][4];
#pragma unroll
  for (int t = 0; t < 4; ++t) {
    float m = sc[0][t][0];
#pragma unroll
    for (int kt = 0; kt < 8; ++kt)
#pragma unroll
      for (int i = 0; i < 4; ++i) m = fmaxf(m, sc[kt][t][i]);
    m = fmaxf(m, __shfl_xor(m, 16));
    m = fmaxf(m, __shfl_xor(m, 32));
    mx[t] = m * CS;
    float l = 0.f;
#pragma unroll
    for (int kt = 0; kt < 8; ++kt)
#pragma unroll
      for (int i = 0; i < 4; ++i) {
        const float p = __builtin_amdgcn_exp2f(fmaf(sc[kt][t][i], CS, -mx[t]));
        sc[kt][t][i] = p;
        l += p;
      }
    l += __shfl_xor(l, 16);
    l += __shfl_xor(l, 32);
    ls[t] = l;
#pragma unroll
    for (int u = 0; u < 4; ++u)
      pf[u][t] = v4u{pk2(sc[2 * u][t][0], sc[2 * u][t][1]), pk2(sc[2 * u][t][2], sc[2 * u][t][3]),
                     pk2(sc[2 * u + 1][t][0], sc[2 * u + 1][t][1]), pk2(sc[2 * u + 1][t][2], sc[2 * u + 1][t][3])};
  }
  LAS float* ml = (LAS float*)(L + 112 * 1024);
  if (g == 0) {
#pragma unroll
    for (int t = 0; t < 4; ++t) {
      ml[(w * 4 + t) * 16 + c] = mx[t];
      ml[256 + (w * 4 + t) * 16 + c] = ls[t];
    }
  }
  STAMP_AT(3);
#pragma unroll
  for (int e = 0; e < 8; ++e)
#pragma unroll
    for (int t = 0; t < 4; ++t) acc[e][t] = f32x4{0, 0, 0, 0};
#pragma unroll
  for (int u = 0; u < 4; ++u) {
#pragma unroll
    for (int e = 0; e < 8; ++e) {
      const v4u a = tr_col(vr[u], e);
#pragma unroll
      for (int t = 0; t < 4; ++t) acc[e][t] = mfma(a, pf[u][t], acc[e][t]);
    }
  }
  STAMP_AT(4);
#pragma unroll
  for (int t = 0; t < 4; ++t) {
    if (t != w) {
      const int slot = (w - t - 1) & 3;
#pragma unroll
      for (int e = 0; e < 8; ++e) red[((t * 3 + slot) * 8 + e) * 64 + lane] = acc[e][t];
    }
  }
  __syncthreads();
  float mw[4], lw[4];
#pragma unroll
  for (int ww = 0; ww < 4; ++ww) {
    const int src = (w + 1 + ww) & 3;
    mw[ww] = ml[(src * 4 + w) * 16 + c];
    lw[ww] = ml[256 + (src * 4 + w) * 16 + c];
  }
  const float M = fmaxf(fmaxf(mw[0], mw[1]), fmaxf(mw[2], mw[3]));
  float al[4], Ls = 0.f;
#pragma unroll
  for (int ww = 0; ww < 4; ++ww) {
    al[ww] = __builtin_amdgcn_exp2f(mw[ww] - M);
    Ls += al[ww] * lw[ww];
  }
  const float inv = 1.0f / Ls;
#pragma unroll
  for (int ww = 0; ww < 4; ++ww) al[ww] *= inv;
#pragma unroll
  for (int e = 0; e < 8; ++e) own[e] = (w == 0 ? acc[e][0] : w == 1 ? acc[e][1] : w == 2 ? acc[e][2] : acc[e][3]) * al[3];
#pragma unroll
  for (int sl = 0; sl < 3; ++sl)
#pragma unroll
    for (int e = 0; e < 8; ++e) own[e] += red[((w * 3 + sl) * 8 + e) * 64 + lane] * al[sl];
  uint16_t* orow = ows + (size_t)(b * SQ + 16 * w + c) * (HQ * DH) + h * DH + 32 * g;
#pragma unroll
  for (int i = 0; i < 4; ++i) {
    v4u p = {pk2(own[0][i], own[1][i]), pk2(own[2][i], own[3][i]), pk2(own[4][i], own[5][i]), pk2(own[6][i], own[7][i])};
    *(v4u*)(orow + 8 * i) = p;
  }
  STAMP_AT(5);
}

__global__ __launch_bounds__(512, 1) void k_oproj(const uint16_t* __restrict__ ows, const uint16_t* __restrict__ wo, uint16_t* __restrict__ out) {
  constexpr int STAMP_BASE = 256;
  STAMP_AT(0);
  __shared__ f32x4 red[8 * 8 * 64];
  const int bid = blockIdx.x;
  const int mt = bid / 9, nt = bid % 9;
  const int w = __builtin_amdgcn_readfirstlane(threadIdx.x >> 6);
  const int lane = threadIdx.x & 63, c = lane & 15, g = lane >> 4;
  const __amdgpu_buffer_rsrc_t ro = __builtin_amdgcn_make_buffer_rsrc((void*)(ows + (size_t)mt * 32 * 4096 + w * 512), 0, 32 * 4096 * 2, RSRC_W3);
  const __amdgpu_buffer_rsrc_t rw = __builtin_amdgcn_make_buffer_rsrc((void*)(wo + (size_t)w * 512 * 576 + nt * 64), 0, 512 * 576 * 2, RSRC_W3);
  const int vo_o = c * 8192 + g * 16;
  const int vo_w = g * 8 * 1152 + c * 8;
  f32x4 acc[4][2];
#pragma unroll
  for (int e = 0; e < 4; ++e) acc[e][0] = acc[e][1] = f32x4{0, 0, 0, 0};
#pragma unroll 4
  for (int s = 0; s < 16; ++s) {
    v2u r[8];
#pragma unroll
    for (int j = 0; j < 8; ++j) r[j] = __builtin_amdgcn_raw_buffer_load_b64(rw, vo_w, (s * 32 + j) * 1152, 0);
    const v4u o0 = ld128(ro, vo_o, s * 64), o1 = ld128(ro, vo_o, 16 * 8192 + s * 64);
#pragma unroll
    for (int e = 0; e < 4; ++e) {
      const unsigned sel = (e & 1) ? 0x07060302u : 0x05040100u;
      v4u a;
      a.x = __builtin_amdgcn_perm(r[1][e >> 1], r[0][e >> 1], sel);
      a.y = __builtin_amdgcn_perm(r[3][e >> 1], r[2][e >> 1], sel);
      a.z = __builtin_amdgcn_perm(r[5][e >> 1], r[4][e >> 1], sel);
      a.w = __builtin_amdgcn_perm(r[7][e >> 1], r[6][e >> 1], sel);
      acc[e][0] = mfma(a, o0, acc[e][0]);
      acc[e][1] = mfma(a, o1, acc[e][1]);
    }
  }
#pragma unroll
  for (int e = 0; e < 4; ++e)
#pragma unroll
    for (int t = 0; t < 2; ++t) red[(w * 8 + e * 2 + t) * 64 + lane] = acc[e][t];
  __syncthreads();
  if (w < 2) {
    f32x4 v[4];
#pragma unroll
    for (int e = 0; e < 4; ++e) {
      v[e] = f32x4{0, 0, 0, 0};
#pragma unroll
      for (int ww = 0; ww < 8; ++ww) v[e] += red[(ww * 8 + e * 2 + w) * 64 + lane];
    }
    uint16_t* orow = out + (size_t)(mt * 32 + w * 16 + c) * 576 + nt * 64 + 16 * g;
    v4u p0 = {pk2(v[0][0], v[1][0]), pk2(v[2][0], v[3][0]), pk2(v[0][1], v[1][1]), pk2(v[2][1], v[3][1])};
    v4u p1 = {pk2(v[0][2], v[1][2]), pk2(v[2][2], v[3][2]), pk2(v[0][3], v[1][3]), pk2(v[2][3], v[3][3])};
    *(v4u*)(orow) = p0;
    *(v4u*)(orow + 8) = p1;
  }
  STAMP_AT(5);
}

extern "C" void kernel_launch(void* const* d_in, const int* in_sizes, int n_in, void* d_out, int out_size, void* d_ws,
                              size_t ws_size, hipStream_t stream) {
  const uint16_t* x = (const uint16_t*)d_in[0];
  const uint16_t* kc = (const uint16_t*)d_in[1];
  const uint16_t* vc = (const uint16_t*)d_in[2];
  const uint16_t* wq = (const uint16_t*)d_in[3];
  const uint16_t* wo = (const uint16_t*)d_in[4];
  uint16_t* ows = (uint16_t*)d_ws;
  (void)hipFuncSetAttribute((const void*)k_attn, hipFuncAttributeMaxDynamicSharedMemorySize, LDS1);
  k_attn<<<256, 256, LDS1, stream>>>(x, kc, vc, wq, ows);
  k_oproj<<<144, 512, 0, stream>>>(ows, wo, (uint16_t*)d_out);
}
```

```cpp
#define B0 0
#define B1 256
#define SI 0
#define AGGMAX 1
#define MULT 0
#include <hip/hip_runtime.h>
#include <hip/hip_bf16.h>
#include <stdint.h>

typedef unsigned v4u __attribute__((ext_vector_type(4)));
typedef unsigned v2u __attribute__((ext_vector_type(2)));
typedef float f32x4 __attribute__((ext_vector_type(4)));
typedef short bf16x8 __attribute__((ext_vector_type(8)));
typedef __bf16 bf2 __attribute__((ext_vector_type(2)));
typedef float f32x2 __attribute__((ext_vector_type(2)));
#define LAS __attribute__((address_space(3)))

constexpr int B_ = 8, SQ = 64, D_ = 576, HQ = 32, HKV = 16, SK = 512, DH = 128;
constexpr int RSRC_W3 = 0x00020000;

__device__ __forceinline__ f32x4 mfma(v4u a, v4u b, f32x4 c) {
  return __builtin_amdgcn_mfma_f32_16x16x32_bf16(__builtin_bit_cast(bf16x8, a), __builtin_bit_cast(bf16x8, b), c, 0, 0, 0);
}
__device__ __forceinline__ unsigned pk2(float lo, float hi) {
  f32x2 v = {lo, hi};
  bf2 r = __builtin_convertvector(v, bf2);
  return __builtin_bit_cast(unsigned, r);
}
__device__ __forceinline__ v4u ld128(__amdgpu_buffer_rsrc_t rs, int voff, int soff) {
  return __builtin_amdgcn_raw_buffer_load_b128(rs, voff, soff, 0);
}
#define SBAR() __builtin_amdgcn_sched_barrier(0)

__device__ __forceinline__ v4u tr_col(const v4u (&r)[8], int e) {
  const unsigned sel = (e & 1) ? 0x07060302u : 0x05040100u;
  v4u o;
  o.x = __builtin_amdgcn_perm(r[1][e >> 1], r[0][e >> 1], sel);
  o.y = __builtin_amdgcn_perm(r[3][e >> 1], r[2][e >> 1], sel);
  o.z = __builtin_amdgcn_perm(r[5][e >> 1], r[4][e >> 1], sel);
  o.w = __builtin_amdgcn_perm(r[7][e >> 1], r[6][e >> 1], sel);
  return o;
}

#define STAMP_AT(i) do { if (threadIdx.x == 0) ((unsigned long long*)((char*)ows + (64 << 20)))[(blockIdx.x + STAMP_BASE) * 16 + (i)] = __builtin_amdgcn_s_memrealtime(); } while (0)
__global__ void k_probe(const unsigned long long* st) {
  __shared__ unsigned long long a[512], z[512];
  const int t = threadIdx.x;
  unsigned long long t0 = t < 256 ? st[t * 16] : ~0ull;
  unsigned long long v = (t >= B0 && t < B1) ? st[t * 16 + SI] : (AGGMAX ? 0ull : ~0ull);
  a[t] = t0; z[t] = v; __syncthreads();
  for (int s = 256; s; s >>= 1) { if (t < s) { a[t] = a[t] < a[t + s] ? a[t] : a[t + s]; z[t] = AGGMAX ? (z[t] > z[t + s] ? z[t] : z[t + s]) : (z[t] < z[t + s] ? z[t] : z[t + s]); } __syncthreads(); }
  unsigned long long d = (z[0] - a[0]) * MULT;
  if (d > 200000) d = 200000;
  const unsigned long long e = __builtin_amdgcn_s_memrealtime() + d;
  while (__builtin_amdgcn_s_memrealtime() < e) __builtin_amdgcn_s_sleep(1);
}

constexpr int LDS1 = 96 * 1024 + 16 * 1024 + 4096;

__global__ __launch_bounds__(256, 1) void k_attn(const uint16_t* __restrict__ x, const uint16_t* __restrict__ kc,
                                                 const uint16_t* __restrict__ vc, const uint16_t* __restrict__ wq,
                                                 uint16_t* __restrict__ ows) {
  constexpr int STAMP_BASE = 0;
  STAMP_AT(0);
  extern __shared__ __attribute__((aligned(16))) char smem[];
  LAS char* L = (LAS char*)smem;
  const int bid = blockIdx.x;
  const int h = 4 * (bid & 7) + ((bid >> 3) & 3);
  const int b = bid >> 5;
  const int kvh = h >> 1;
  const int w = __builtin_amdgcn_readfirstlane(threadIdx.x >> 6);
  const int lane = threadIdx.x & 63, c = lane & 15, g = lane >> 4;

  const __amdgpu_buffer_rsrc_t rx = __builtin_amdgcn_make_buffer_rsrc((void*)(x + (size_t)b * SQ * D_), 0, SQ * D_ * 2, RSRC_W3);
  const __amdgpu_buffer_rsrc_t rw = __builtin_amdgcn_make_buffer_rsrc((void*)(wq + h * DH), 0, (D_ * HQ * DH - h * DH) * 2, RSRC_W3);
  const __amdgpu_buffer_rsrc_t rk = __builtin_amdgcn_make_buffer_rsrc((void*)(kc + ((size_t)(b * HKV + kvh) * SK + 128 * w) * DH), 0, 128 * DH * 2, RSRC_W3);
  const __amdgpu_buffer_rsrc_t rv = __builtin_amdgcn_make_buffer_rsrc((void*)(vc + ((size_t)(b * HKV + kvh) * SK + 128 * w) * DH), 0, 128 * DH * 2, RSRC_W3);

  const int vo_w = g * 8 * 8192 + c * 16;
  const int vo_x = c * 1152 + g * 16;
  const int vo_k = c * 256 + g * 16;
  const int vo_v = g * 1024 + c * 16;

  v4u wr[3][8], xf[3][4];
#define LOADQ(i, slot) do { const int s_ = w + 4 * (i); const int sx_ = s_ < 18 ? s_ : 0; \
    _Pragma("unroll") for (int j = 0; j < 8; ++j) wr[slot][j] = ld128(rw, vo_w, (s_ * 32 + j) * 8192); \
    _Pragma("unroll") for (int t = 0; t < 4; ++t) xf[slot][t] = ld128(rx, vo_x, t * 16 * 1152 + sx_ * 64); SBAR(); } while (0)
#define QSTEP(slot) do { _Pragma("unroll") for (int e = 0; e < 8; ++e) { const v4u a = tr_col(wr[slot], e); \
    _Pragma("unroll") for (int t = 0; t < 4; ++t) acc[e][t] = mfma(a, xf[slot][t], acc[e][t]); } SBAR(); } while (0)
  LOADQ(0, 0); LOADQ(1, 1); LOADQ(2, 2);
  v4u kf[8][4];
#pragma unroll
  for (int kt = 0; kt < 4; ++kt)
#pragma unroll
    for (int s = 0; s < 4; ++s) kf[kt][s] = ld128(rk, vo_k + s * 64, kt * 4096);
  SBAR();

  f32x4 acc[8][4];
#pragma unroll
  for (int e = 0; e < 8; ++e)
#pragma unroll
    for (int t = 0; t < 4; ++t) acc[e][t] = f32x4{0, 0, 0, 0};
  QSTEP(0); LOADQ(3, 0);
  QSTEP(1); LOADQ(4, 1);
#pragma unroll
  for (int kt = 4; kt < 8; ++kt)
#pragma unroll
    for (int s = 0; s < 4; ++s) kf[kt][s] = ld128(rk, vo_k + s * 64, kt * 4096);
  SBAR();
  QSTEP(2); QSTEP(0); QSTEP(1);
  v4u vr[4][8];
#pragma unroll
  for (int u = 0; u < 4; ++u)
#pragma unroll
    for (int j = 0; j < 8; ++j) vr[u][j] = ld128(rv, vo_v + (j & 3) * 256, u * 8192 + (j >> 2) * 4096);
  SBAR();
  STAMP_AT(1);

  LAS f32x4* red = (LAS f32x4*)L;
#pragma unroll
  for (int t = 0; t < 4; ++t) {
    if (t != w) {
      const int slot = (w - t - 1) & 3;
#pragma unroll
      for (int e = 0; e < 8; ++e) red[((t * 3 + slot) * 8 + e) * 64 + lane] = acc[e][t];
    }
  }
  __syncthreads();
  f32x4 own[8];
#pragma unroll
  for (int e = 0; e < 8; ++e) own[e] = w == 0 ? acc[e][0] : w == 1 ? acc[e][1] : w == 2 ? acc[e][2] : acc[e][3];
#pragma unroll
  for (int sl = 0; sl < 3; ++sl)
#pragma unroll
    for (int e = 0; e < 8; ++e) own[e] += red[((w * 3 + sl) * 8 + e) * 64 + lane];
  LAS v4u* qf = (LAS v4u*)(L + 96 * 1024);
#pragma unroll
  for (int i = 0; i < 4; ++i) {
    v4u p = {pk2(own[0][i], own[1][i]), pk2(own[2][i], own[3][i]), pk2(own[4][i], own[5][i]), pk2(own[6][i], own[7][i])};
    qf[(w * 4 + g) * 64 + i * 16 + c] = p;
  }
  __syncthreads();
  v4u q[4][4];
#pragma unroll
  for (int t = 0; t < 4; ++t)
#pragma unroll
    for (int s = 0; s < 4; ++s) q[t][s] = qf[(t * 4 + s) * 64 + lane];
  STAMP_AT(2);

  f32x4 sc[8][4];
#pragma unroll
  for (int kt = 0; kt < 8; ++kt)
#pragma unroll
    for (int t = 0; t < 4; ++t) {
      f32x4 a = {0, 0, 0, 0};
#pragma unroll
      for (int s = 0; s < 4; ++s) a = mfma(kf[kt][s], q[t][s], a);
      sc[kt][t] = a;
    }
  constexpr float CS = 0.088388347648318447f * 1.4426950408889634f;
  float mx[4], ls[4];
  v4u pf[4][4];
#pragma unroll
  for (int t = 0; t < 4; ++t) {
    float m = sc[0][t][0];
#pragma unroll
    for (int kt = 0; kt < 8; ++kt)
#pragma unroll
      for (int i = 0; i < 4; ++i) m = fmaxf(m, sc[kt][t][i]);
    m = fmaxf(m, __shfl_xor(m, 16));
    m = fmaxf(m, __shfl_xor(m, 32));
    mx[t] = m * CS;
    float l = 0.f;
#pragma unroll
    for (int kt = 0; kt < 8; ++kt)
#pragma unroll
      for (int i = 0; i < 4; ++i) {
        const float p = __builtin_amdgcn_exp2f(fmaf(sc[kt][t][i], CS, -mx[t]));
        sc[kt][t][i] = p;
        l += p;
      }
    l += __shfl_xor(l, 16);
    l += __shfl_xor(l, 32);
    ls[t] = l;
#pragma unroll
    for (int u = 0; u < 4; ++u)
      pf[u][t] = v4u{pk2(sc[2 * u][t][0], sc[2 * u][t][1]), pk2(sc[2 * u][t][2], sc[2 * u][t][3]),
                     pk2(sc[2 * u + 1][t][0], sc[2 * u + 1][t][1]), pk2(sc[2 * u + 1][t][2], sc[2 * u + 1][t][3])};
  }
  LAS float* ml = (LAS float*)(L + 112 * 1024);
  if (g == 0) {
#pragma unroll
    for (int t = 0; t < 4; ++t) {
      ml[(w * 4 + t) * 16 + c] = mx[t];
      ml[256 + (w * 4 + t) * 16 + c] = ls[t];
    }
  }
  STAMP_AT(3);
#pragma unroll
  for (int e = 0; e < 8; ++e)
#pragma unroll
    for (int t = 0; t < 4; ++t) acc[e][t] = f32x4{0, 0, 0, 0};
#pragma unroll
  for (int u = 0; u < 4; ++u) {
#pragma unroll
    for (int e = 0; e < 8; ++e) {
      const v4u a = tr_col(vr[u], e);
#pragma unroll
      for (int t = 0; t < 4; ++t) acc[e][t] = mfma(a, pf[u][t], acc[e][t]);
    }
  }
  STAMP_AT(4);
#pragma unroll
  for (int t = 0; t < 4; ++t) {
    if (t != w) {
      const int slot = (w - t - 1) & 3;
#pragma unroll
      for (int e = 0; e < 8; ++e) red[((t * 3 + slot) * 8 + e) * 64 + lane] = acc[e][t];
    }
  }
  __syncthreads();
  float mw[4], lw[4];
#pragma unroll
  for (int ww = 0; ww < 4; ++ww) {
    const int src = (w + 1 + ww) & 3;
    mw[ww] = ml[(src * 4 + w) * 16 + c];
    lw[ww] = ml[256 + (src * 4 + w) * 16 + c];
  }
  const float M = fmaxf(fmaxf(mw[0], mw[1]), fmaxf(mw[2], mw[3]));
  float al[4], Ls = 0.f;
#pragma unroll
  for (int ww = 0; ww < 4; ++ww) {
    al[ww] = __builtin_amdgcn_exp2f(mw[ww] - M);
    Ls += al[ww] * lw[ww];
  }
  const float inv = 1.0f / Ls;
#pragma unroll
  for (int ww = 0; ww < 4; ++ww) al[ww] *= inv;
#pragma unroll
  for (int e = 0; e < 8; ++e) own[e] = (w == 0 ? acc[e][0] : w == 1 ? acc[e][1] : w == 2 ? acc[e][2] : acc[e][3]) * al[3];
#pragma unroll
  for (int sl = 0; sl < 3; ++sl)
#pragma unroll
    for (int e = 0; e < 8; ++e) own[e] += red[((w * 3 + sl) * 8 + e) * 64 + lane] * al[sl];
  uint16_t* orow = ows + (size_t)(b * SQ + 16 * w + c) * (HQ * DH) + h * DH + 32 * g;
#pragma unroll
  for (int i = 0; i < 4; ++i) {
    v4u p = {pk2(own[0][i], own[1][i]), pk2(own[2][i], own[3][i]), pk2(own[4][i], own[5][i]), pk2(own[6][i], own[7][i])};
    *(v4u*)(orow + 8 * i) = p;
  }
  STAMP_AT(5);
}

__global__ __launch_bounds__(512, 1) void k_oproj(const uint16_t* __restrict__ ows, const uint16_t* __restrict__ wo, uint16_t* __restrict__ out) {
  constexpr int STAMP_BASE = 256;
  STAMP_AT(0);
  __shared__ f32x4 red[8 * 8 * 64];
  const int bid = blockIdx.x;
  const int mt = bid / 9, nt = bid % 9;
  const int w = __builtin_amdgcn_readfirstlane(threadIdx.x >> 6);
  const int lane = threadIdx.x & 63, c = lane & 15, g = lane >> 4;
  const __amdgpu_buffer_rsrc_t ro = __builtin_amdgcn_make_buffer_rsrc((void*)(ows + (size_t)mt * 32 * 4096 + w * 512), 0, 32 * 4096 * 2, RSRC_W3);
  const __amdgpu_buffer_rsrc_t rw = __builtin_amdgcn_make_buffer_rsrc((void*)(wo + (size_t)w * 512 * 576 + nt * 64), 0, 512 * 576 * 2, RSRC_W3);
  const int vo_o = c * 8192 + g * 16;
  const int vo_w = g * 8 * 1152 + c * 8;
  f32x4 acc[4][2];
#pragma unroll
  for (int e = 0; e < 4; ++e) acc[e][0] = acc[e][1] = f32x4{0, 0, 0, 0};
#pragma unroll 4
  for (int s = 0; s < 16; ++s) {
    v2u r[8];
#pragma unroll
    for (int j = 0; j < 8; ++j) r[j] = __builtin_amdgcn_raw_buffer_load_b64(rw, vo_w, (s * 32 + j) * 1152, 0);
    const v4u o0 = ld128(ro, vo_o, s * 64), o1 = ld128(ro, vo_o, 16 * 8192 + s * 64);
#pragma unroll
    for (int e = 0; e < 4; ++e) {
      const unsigned sel = (e & 1) ? 0x07060302u : 0x05040100u;
      v4u a;
      a.x = __builtin_amdgcn_perm(r[1][e >> 1], r[0][e >> 1], sel);
      a.y = __builtin_amdgcn_perm(r[3][e >> 1], r[2][e >> 1], sel);
      a.z = __builtin_amdgcn_perm(r[5][e >> 1], r[4][e >> 1], sel);
      a.w = __builtin_amdgcn_perm(r[7][e >> 1], r[6][e >> 1], sel);
      acc[e][0] = mfma(a, o0, acc[e][0]);
      acc[e][1] = mfma(a, o1, acc[e][1]);
    }
  }
#pragma unroll
  for (int e = 0; e < 4; ++e)
#pragma unroll
    for (int t = 0; t < 2; ++t) red[(w * 8 + e * 2 + t) * 64 + lane] = acc[e][t];
  __syncthreads();
  if (w < 2) {
    f32x4 v[4];
#pragma unroll
    for (int e = 0; e < 4; ++e) {
      v[e] = f32x4{0, 0, 0, 0};
#pragma unroll
      for (int ww = 0; ww < 8; ++ww) v[e] += red[(ww * 8 + e * 2 + w) * 64 + lane];
    }
    uint16_t* orow = out + (size_t)(mt * 32 + w * 16 + c) * 576 + nt * 64 + 16 * g;
    v4u p0 = {pk2(v[0][0], v[1][0]), pk2(v[2][0], v[3][0]), pk2(v[0][1], v[1][1]), pk2(v[2][1], v[3][1])};
    v4u p1 = {pk2(v[0][2], v[1][2]), pk2(v[2][2], v[3][2]), pk2(v[0][3], v[1][3]), pk2(v[2][3], v[3][3])};
    *(v4u*)(orow) = p0;
    *(v4u*)(orow + 8) = p1;
  }
  STAMP_AT(5);
}

extern "C" void kernel_launch(void* const* d_in, const int* in_sizes, int n_in, void* d_out, int out_size, void* d_ws,
                              size_t ws_size, hipStream_t stream) {
  const uint16_t* x = (const uint16_t*)d_in[0];
  const uint16_t* kc = (const uint16_t*)d_in[1];
  const uint16_t* vc = (const uint16_t*)d_in[2];
  const uint16_t* wq = (const uint16_t*)d_in[3];
  const uint16_t* wo = (const uint16_t*)d_in[4];
  uint16_t* ows = (uint16_t*)d_ws;
  (void)hipFuncSetAttribute((const void*)k_attn, hipFuncAttributeMaxDynamicSharedMemorySize, LDS1);
  k_attn<<<256, 256, LDS1, stream>>>(x, kc, vc, wq, ows);
  k_oproj<<<144, 512, 0, stream>>>(ows, wo, (uint16_t*)d_out);
  k_probe<<<1, 512, 0, stream>>>((const unsigned long long*)((char*)d_ws + (64 << 20)));
}
```

```cpp
#define TINY 0
#define EXPR now - a[0]
#define MULT 0
#include <hip/hip_runtime.h>
#include <hip/hip_bf16.h>
#include <stdint.h>

typedef unsigned v4u __attribute__((ext_vector_type(4)));
typedef unsigned v2u __attribute__((ext_vector_type(2)));
typedef float f32x4 __attribute__((ext_vector_type(4)));
typedef short bf16x8 __attribute__((ext_vector_type(8)));
typedef __bf16 bf2 __attribute__((ext_vector_type(2)));
typedef float f32x2 __attribute__((ext_vector_type(2)));
#define LAS __attribute__((address_space(3)))

constexpr int B_ = 8, SQ = 64, D_ = 576, HQ = 32, HKV = 16, SK = 512, DH = 128;
constexpr int RSRC_W3 = 0x00020000;

__device__ __forceinline__ f32x4 mfma(v4u a, v4u b, f32x4 c) {
  return __builtin_amdgcn_mfma_f32_16x16x32_bf16(__builtin_bit_cast(bf16x8, a), __builtin_bit_cast(bf16x8, b), c, 0, 0, 0);
}
__device__ __forceinline__ unsigned pk2(float lo, float hi) {
  f32x2 v = {lo, hi};
  bf2 r = __builtin_convertvector(v, bf2);
  return __builtin_bit_cast(unsigned, r);
}
__device__ __forceinline__ v4u ld128(__amdgpu_buffer_rsrc_t rs, int voff, int soff) {
  return __builtin_amdgcn_raw_buffer_load_b128(rs, voff, soff, 0);
}
#define SBAR() __builtin_amdgcn_sched_barrier(0)

__device__ __forceinline__ v4u tr_col(const v4u (&r)[8], int e) {
  const unsigned sel = (e & 1) ? 0x07060302u : 0x05040100u;
  v4u o;
  o.x = __builtin_amdgcn_perm(r[1][e >> 1], r[0][e >> 1], sel);
  o.y = __builtin_amdgcn_perm(r[3][e >> 1], r[2][e >> 1], sel);
  o.z = __builtin_amdgcn_perm(r[5][e >> 1], r[4][e >> 1], sel);
  o.w = __builtin_amdgcn_perm(r[7][e >> 1], r[6][e >> 1], sel);
  return o;
}

#define STAMP_AT(i) do { if (threadIdx.x == 0) ((unsigned long long*)((char*)ows + (64 << 20)))[(blockIdx.x + STAMP_BASE) * 16 + (i)] = __builtin_amdgcn_s_memrealtime(); } while (0)
__global__ void k_tiny(unsigned long long* st) { const unsigned long long t = __builtin_amdgcn_s_memrealtime(); if (threadIdx.x == 0 && blockIdx.x == 0) st[1000 * 16] = t; }
__global__ void k_probe(const unsigned long long* st) {
  const unsigned long long tp = __builtin_amdgcn_s_memrealtime();
  __shared__ unsigned long long a[512];
  const int t = threadIdx.x;
  unsigned long long t0 = t < 256 ? st[t * 16] : ~0ull;
  const unsigned long long tt = st[1000 * 16];
  a[t] = t0; __syncthreads();
  for (int s = 256; s; s >>= 1) { if (t < s) { a[t] = a[t] < a[t + s] ? a[t] : a[t + s]; } __syncthreads(); }
  const unsigned long long now = __builtin_amdgcn_s_memrealtime();
  unsigned long long d = (EXPR) * MULT;
  if (d > 200000) d = 200000;
  const unsigned long long e = now + d;
  while (__builtin_amdgcn_s_memrealtime() < e) __builtin_amdgcn_s_sleep(1);
}

constexpr int LDS1 = 96 * 1024 + 16 * 1024 + 4096;

__global__ __launch_bounds__(256, 1) void k_attn(const uint16_t* __restrict__ x, const uint16_t* __restrict__ kc,
                                                 const uint16_t* __restrict__ vc, const uint16_t* __restrict__ wq,
                                                 uint16_t* __restrict__ ows) {
  constexpr int STAMP_BASE = 0;
  STAMP_AT(0);
  extern __shared__ __attribute__((aligned(16))) char smem[];
  LAS char* L = (LAS char*)smem;
  const int bid = blockIdx.x;
  const int h = 4 * (bid & 7) + ((bid >> 3) & 3);
  const int b = bid >> 5;
  const int kvh = h >> 1;
  const int w = __builtin_amdgcn_readfirstlane(threadIdx.x >> 6);
  const int lane = threadIdx.x & 63, c = lane & 15, g = lane >> 4;

  const __amdgpu_buffer_rsrc_t rx = __builtin_amdgcn_make_buffer_rsrc((void*)(x + (size_t)b * SQ * D_), 0, SQ * D_ * 2, RSRC_W3);
  const __amdgpu_buffer_rsrc_t rw = __builtin_amdgcn_make_buffer_rsrc((void*)(wq + h * DH), 0, (D_ * HQ * DH - h * DH) * 2, RSRC_W3);
  const __amdgpu_buffer_rsrc_t rk = __builtin_amdgcn_make_buffer_rsrc((void*)(kc + ((size_t)(b * HKV + kvh) * SK + 128 * w) * DH), 0, 128 * DH * 2, RSRC_W3);
  const __amdgpu_buffer_rsrc_t rv = __builtin_amdgcn_make_buffer_rsrc((void*)(vc + ((size_t)(b * HKV + kvh) * SK + 128 * w) * DH), 0, 128 * DH * 2, RSRC_W3);

  const int vo_w = g * 8 * 8192 + c * 16;
  const int vo_x = c * 1152 + g * 16;
  const int vo_k = c * 256 + g * 16;
  const int vo_v = g * 1024 + c * 16;

  v4u wr[3][8], xf[3][4];
#define LOADQ(i, slot) do { const int s_ = w + 4 * (i); const int sx_ = s_ < 18 ? s_ : 0; \
    _Pragma("unroll") for (int j = 0; j < 8; ++j) wr[slot][j] = ld128(rw, vo_w, (s_ * 32 + j) * 8192); \
    _Pragma("unroll") for (int t = 0; t < 4; ++t) xf[slot][t] = ld128(rx, vo_x, t * 16 * 1152 + sx_ * 64); SBAR(); } while (0)
#define QSTEP(slot) do { _Pragma("unroll") for (int e = 0; e < 8; ++e) { const v4u a = tr_col(wr[slot], e); \
    _Pragma("unroll") for (int t = 0; t < 4; ++t) acc[e][t] = mfma(a, xf[slot][t], acc[e][t]); } SBAR(); } while (0)
  LOADQ(0, 0); LOADQ(1, 1); LOADQ(2, 2);
  v4u kf[8][4];
#pragma unroll
  for (int kt = 0; kt < 4; ++kt)
#pragma unroll
    for (int s = 0; s < 4; ++s) kf[kt][s] = ld128(rk, vo_k + s * 64, kt * 4096);
  SBAR();

  f32x4 acc[8][4];
#pragma unroll
  for (int e = 0; e < 8; ++e)
#pragma unroll
    for (int t = 0; t < 4; ++t) acc[e][t] = f32x4{0, 0, 0, 0};
  QSTEP(0); LOADQ(3, 0);
  QSTEP(1); LOADQ(4, 1);
#pragma unroll
  for (int kt = 4; kt < 8; ++kt)
#pragma unroll
    for (int s = 0; s < 4; ++s) kf[kt][s] = ld128(rk, vo_k + s * 64, kt * 4096);
  SBAR();
  QSTEP(2); QSTEP(0); QSTEP(1);
  v4u vr[4][8];
#pragma unroll
  for (int u = 0; u < 4; ++u)
#pragma unroll
    for (int j = 0; j < 8; ++j) vr[u][j] = ld128(rv, vo_v + (j & 3) * 256, u * 8192 + (j >> 2) * 4096);
  SBAR();
  STAMP_AT(1);

  LAS f32x4* red = (LAS f32x4*)L;
#pragma unroll
  for (int t = 0; t < 4; ++t) {
    if (t != w) {
      const int slot = (w - t - 1) & 3;
#pragma unroll
      for (int e = 0; e < 8; ++e) red[((t * 3 + slot) * 8 + e) * 64 + lane] = acc[e][t];
    }
  }
  __syncthreads();
  f32x4 own[8];
#pragma unroll
  for (int e = 0; e < 8; ++e) own[e] = w == 0 ? acc[e][0] : w == 1 ? acc[e][1] : w == 2 ? acc[e][2] : acc[e][3];
#pragma unroll
  for (int sl = 0; sl < 3; ++sl)
#pragma unroll
    for (int e = 0; e < 8; ++e) own[e] += red[((w * 3 + sl) * 8 + e) * 64 + lane];
  LAS v4u* qf = (LAS v4u*)(L + 96 * 1024);
#pragma unroll
  for (int i = 0; i < 4; ++i) {
    v4u p = {pk2(own[0][i], own[1][i]), pk2(own[2][i], own[3][i]), pk2(own[4][i], own[5][i]), pk2(own[6][i], own[7][i])};
    qf[(w * 4 + g) * 64 + i * 16 + c] = p;
  }
  __syncthreads();
  v4u q[4][4];
#pragma unroll
  for (int t = 0; t < 4; ++t)
#pragma unroll
    for (int s = 0; s < 4; ++s) q[t][s] = qf[(t * 4 + s) * 64 + lane];
  STAMP_AT(2);

  f32x4 sc[8][4];
#pragma unroll
  for (int kt = 0; kt < 8; ++kt)
#pragma unroll
    for (int t = 0; t < 4; ++t) {
      f32x4 a = {0, 0, 0, 0};
#pragma unroll
      for (int s = 0; s < 4; ++s) a = mfma(kf[kt][s], q[t][s], a);
      sc[kt][t] = a;
    }
  constexpr float CS = 0.088388347648318447f * 1.4426950408889634f;
  float mx[4], ls[4];
  v4u pf[4][4];
#pragma unroll
  for (int t = 0; t < 4; ++t) {
    float m = sc[0][t][0];
#pragma unroll
    for (int kt = 0; kt < 8; ++kt)
#pragma unroll
      for (int i = 0; i < 4; ++i) m = fmaxf(m, sc[kt][t][i]);
    m = fmaxf(m, __shfl_xor(m, 16));
    m = fmaxf(m, __shfl_xor(m, 32));
    mx[t] = m * CS;
    float l = 0.f;
#pragma unroll
    for (int kt = 0; kt < 8; ++kt)
#pragma unroll
      for (int i = 0; i < 4; ++i) {
        const float p = __builtin_amdgcn_exp2f(fmaf(sc[kt][t][i], CS, -mx[t]));
        sc[kt][t][i] = p;
        l += p;
      }
    l += __shfl_xor(l, 16);
    l += __shfl_xor(l, 32);
    ls[t] = l;
#pragma unroll
    for (int u = 0; u < 4; ++u)
      pf[u][t] = v4u{pk2(sc[2 * u][t][0], sc[2 * u][t][1]), pk2(sc[2 * u][t][2], sc[2 * u][t][3]),
                     pk2(sc[2 * u + 1][t][0], sc[2 * u + 1][t][1]), pk2(sc[2 * u + 1][t][2], sc[2 * u + 1][t][3])};
  }
  LAS float* ml = (LAS float*)(L + 112 * 1024);
  if (g == 0) {
#pragma unroll
    for (int t = 0; t < 4; ++t) {
      ml[(w * 4 + t) * 16 + c] = mx[t];
      ml[256 + (w * 4 + t) * 16 + c] = ls[t];
    }
  }
  STAMP_AT(3);
#pragma unroll
  for (int e = 0; e < 8; ++e)
#pragma unroll
    for (int t = 0; t < 4; ++t) acc[e][t] = f32x4{0, 0, 0, 0};
#pragma unroll
  for (int u = 0; u < 4; ++u) {
#pragma unroll
    for (int e = 0; e < 8; ++e) {
      const v4u a = tr_col(vr[u], e);
#pragma unroll
      for (int t = 0; t < 4; ++t) acc[e][t] = mfma(a, pf[u][t], acc[e][t]);
    }
  }
  STAMP_AT(4);
#pragma unroll
  for (int t = 0; t < 4; ++t) {
    if (t != w) {
      const int slot = (w - t - 1) & 3;
#pragma unroll
      for (int e = 0; e < 8; ++e) red[((t * 3 + slot) * 8 + e) * 64 + lane] = acc[e][t];
    }
  }
  __syncthreads();
  float mw[4], lw[4];
#pragma unroll
  for (int ww = 0; ww < 4; ++ww) {
    const int src = (w + 1 + ww) & 3;
    mw[ww] = ml[(src * 4 + w) * 16 + c];
    lw[ww] = ml[256 + (src * 4 + w) * 16 + c];
  }
  const float M = fmaxf(fmaxf(mw[0], mw[1]), fmaxf(mw[2], mw[3]));
  float al[4], Ls = 0.f;
#pragma unroll
  for (int ww = 0; ww < 4; ++ww) {
    al[ww] = __builtin_amdgcn_exp2f(mw[ww] - M);
    Ls += al[ww] * lw[ww];
  }
  const float inv = 1.0f / Ls;
#pragma unroll
  for (int ww = 0; ww < 4; ++ww) al[ww] *= inv;
#pragma unroll
  for (int e = 0; e < 8; ++e) own[e] = (w == 0 ? acc[e][0] : w == 1 ? acc[e][1] : w == 2 ? acc[e][2] : acc[e][3]) * al[3];
#pragma unroll
  for (int sl = 0; sl < 3; ++sl)
#pragma unroll
    for (int e = 0; e < 8; ++e) own[e] += red[((w * 3 + sl) * 8 + e) * 64 + lane] * al[sl];
  uint16_t* orow = ows + (size_t)(b * SQ + 16 * w + c) * (HQ * DH) + h * DH + 32 * g;
#pragma unroll
  for (int i = 0; i < 4; ++i) {
    v4u p = {pk2(own[0][i], own[1][i]), pk2(own[2][i], own[3][i]), pk2(own[4][i], own[5][i]), pk2(own[6][i], own[7][i])};
    *(v4u*)(orow + 8 * i) = p;
  }
  STAMP_AT(5);
}

__global__ __launch_bounds__(512, 1) void k_oproj(const uint16_t* __restrict__ ows, const uint16_t* __restrict__ wo, uint16_t* __restrict__ out) {
  constexpr int STAMP_BASE = 256;
  STAMP_AT(0);
  __shared__ f32x4 red[8 * 8 * 64];
  const int bid = blockIdx.x;
  const int mt = bid / 9, nt = bid % 9;
  const int w = __builtin_amdgcn_readfirstlane(threadIdx.x >> 6);
  const int lane = threadIdx.x & 63, c = lane & 15, g = lane >> 4;
  const __amdgpu_buffer_rsrc_t ro = __builtin_amdgcn_make_buffer_rsrc((void*)(ows + (size_t)mt * 32 * 4096 + w * 512), 0, 32 * 4096 * 2, RSRC_W3);
  const __amdgpu_buffer_rsrc_t rw = __builtin_amdgcn_make_buffer_rsrc((void*)(wo + (size_t)w * 512 * 576 + nt * 64), 0, 512 * 576 * 2, RSRC_W3);
  const int vo_o = c * 8192 + g * 16;
  const int vo_w = g * 8 * 1152 + c * 8;
  f32x4 acc[4][2];
#pragma unroll
  for (int e = 0; e < 4; ++e) acc[e][0] = acc[e][1] = f32x4{0, 0, 0, 0};
#pragma unroll 4
  for (int s = 0; s < 16; ++s) {
    v2u r[8];
#pragma unroll
    for (int j = 0; j < 8; ++j) r[j] = __builtin_amdgcn_raw_buffer_load_b64(rw, vo_w, (s * 32 + j) * 1152, 0);
    const v4u o0 = ld128(ro, vo_o, s * 64), o1 = ld128(ro, vo_o, 16 * 8192 + s * 64);
#pragma unroll
    for (int e = 0; e < 4; ++e) {
      const unsigned sel = (e & 1) ? 0x07060302u : 0x05040100u;
      v4u a;
      a.x = __builtin_amdgcn_perm(r[1][e >> 1], r[0][e >> 1], sel);
      a.y = __builtin_amdgcn_perm(r[3][e >> 1], r[2][e >> 1], sel);
      a.z = __builtin_amdgcn_perm(r[5][e >> 1], r[4][e >> 1], sel);
      a.w = __builtin_amdgcn_perm(r[7][e >> 1], r[6][e >> 1], sel);
      acc[e][0] = mfma(a, o0, acc[e][0]);
      acc[e][1] = mfma(a, o1, acc[e][1]);
    }
  }
#pragma unroll
  for (int e = 0; e < 4; ++e)
#pragma unroll
    for (int t = 0; t < 2; ++t) red[(w * 8 + e * 2 + t) * 64 + lane] = acc[e][t];
  __syncthreads();
  if (w < 2) {
    f32x4 v[4];
#pragma unroll
    for (int e = 0; e < 4; ++e) {
      v[e] = f32x4{0, 0, 0, 0};
#pragma unroll
      for (int ww = 0; ww < 8; ++ww) v[e] += red[(ww * 8 + e * 2 + w) * 64 + lane];
    }
    uint16_t* orow = out + (size_t)(mt * 32 + w * 16 + c) * 576 + nt * 64 + 16 * g;
    v4u p0 = {pk2(v[0][0], v[1][0]), pk2(v[2][0], v[3][0]), pk2(v[0][1], v[1][1]), pk2(v[2][1], v[3][1])};
    v4u p1 = {pk2(v[0][2], v[1][2]), pk2(v[2][2], v[3][2]), pk2(v[0][3], v[1][3]), pk2(v[2][3], v[3][3])};
    *(v4u*)(orow) = p0;
    *(v4u*)(orow + 8) = p1;
  }
  STAMP_AT(5);
}

extern "C" void kernel_launch(void* const* d_in, const int* in_sizes, int n_in, void* d_out, int out_size, void* d_ws,
                              size_t ws_size, hipStream_t stream) {
  const uint16_t* x = (const uint16_t*)d_in[0];
  const uint16_t* kc = (const uint16_t*)d_in[1];
  const uint16_t* vc = (const uint16_t*)d_in[2];
  const uint16_t* wq = (const uint16_t*)d_in[3];
  const uint16_t* wo = (const uint16_t*)d_in[4];
  uint16_t* ows = (uint16_t*)d_ws;
  (void)hipFuncSetAttribute((const void*)k_attn, hipFuncAttributeMaxDynamicSharedMemorySize, LDS1);
  if (TINY) k_tiny<<<TINY, 64, 0, stream>>>((unsigned long long*)((char*)d_ws + (64 << 20)));
  k_attn<<<256, 256, LDS1, stream>>>(x, kc, vc, wq, ows);
  k_oproj<<<144, 512, 0, stream>>>(ows, wo, (uint16_t*)d_out);
  k_probe<<<1, 512, 0, stream>>>((const unsigned long long*)((char*)d_ws + (64 << 20)));
}
```

```cpp
#define AUXV 0
#define MULT 0
#define PA kc
#define PB vc
#define PW 16384
#include <hip/hip_runtime.h>
#include <hip/hip_bf16.h>
#include <stdint.h>

typedef unsigned v4u __attribute__((ext_vector_type(4)));
typedef unsigned v2u __attribute__((ext_vector_type(2)));
typedef float f32x4 __attribute__((ext_vector_type(4)));
typedef short bf16x8 __attribute__((ext_vector_type(8)));
typedef __bf16 bf2 __attribute__((ext_vector_type(2)));
typedef float f32x2 __attribute__((ext_vector_type(2)));
#define LAS __attribute__((address_space(3)))

constexpr int B_ = 8, SQ = 64, D_ = 576, HQ = 32, HKV = 16, SK = 512, DH = 128;
constexpr int RSRC_W3 = 0x00020000;

__device__ __forceinline__ f32x4 mfma(v4u a, v4u b, f32x4 c) {
  return __builtin_amdgcn_mfma_f32_16x16x32_bf16(__builtin_bit_cast(bf16x8, a), __builtin_bit_cast(bf16x8, b), c, 0, 0, 0);
}
__device__ __forceinline__ unsigned pk2(float lo, float hi) {
  f32x2 v = {lo, hi};
  bf2 r = __builtin_convertvector(v, bf2);
  return __builtin_bit_cast(unsigned, r);
}
__device__ __forceinline__ v4u ld128(__amdgpu_buffer_rsrc_t rs, int voff, int soff) {
  return __builtin_amdgcn_raw_buffer_load_b128(rs, voff, soff, 0);
}
#define SBAR() __builtin_amdgcn_sched_barrier(0)

__device__ __forceinline__ v4u tr_col(const v4u (&r)[8], int e) {
  const unsigned sel = (e & 1) ? 0x07060302u : 0x05040100u;
  v4u o;
  o.x = __builtin_amdgcn_perm(r[1][e >> 1], r[0][e >> 1], sel);
  o.y = __builtin_amdgcn_perm(r[3][e >> 1], r[2][e >> 1], sel);
  o.z = __builtin_amdgcn_perm(r[5][e >> 1], r[4][e >> 1], sel);
  o.w = __builtin_amdgcn_perm(r[7][e >> 1], r[6][e >> 1], sel);
  return o;
}

#define STAMP_AT(i)
__global__ __launch_bounds__(256, 1) void k_stream(const char* A, const char* Bp, unsigned long long* st, int per_wave) {
  const unsigned long long t0 = __builtin_amdgcn_s_memrealtime();
  const int w = __builtin_amdgcn_readfirstlane(threadIdx.x >> 6), lane = threadIdx.x & 63;
  const __amdgpu_buffer_rsrc_t ra = __builtin_amdgcn_make_buffer_rsrc((void*)(A + ((size_t)blockIdx.x * 4 + w) * per_wave), 0, per_wave, RSRC_W3);
  const __amdgpu_buffer_rsrc_t rb = __builtin_amdgcn_make_buffer_rsrc((void*)(Bp + ((size_t)blockIdx.x * 4 + w) * per_wave), 0, per_wave, RSRC_W3);
  v4u r[32];
#pragma unroll
  for (int i = 0; i < 16; ++i) r[i] = __builtin_amdgcn_raw_buffer_load_b128(ra, lane * 16, i * 1024, AUXV);
#pragma unroll
  for (int i = 0; i < 16; ++i) r[16 + i] = __builtin_amdgcn_raw_buffer_load_b128(rb, lane * 16, i * 1024, AUXV);
  v4u a = r[0];
#pragma unroll
  for (int i = 1; i < 32; ++i) a ^= r[i];
  asm volatile("" :: "v"(a));
  __syncthreads();
  const unsigned long long t1 = __builtin_amdgcn_s_memrealtime();
  if (threadIdx.x == 0) { st[blockIdx.x * 16] = t0; st[blockIdx.x * 16 + 1] = t1; }
}
__global__ void k_probe(const unsigned long long* st) {
  __shared__ unsigned long long a[256], z[256];
  const int t = threadIdx.x;
  a[t] = st[t * 16]; z[t] = st[t * 16 + 1]; __syncthreads();
  for (int s = 128; s; s >>= 1) { if (t < s) { a[t] = a[t] < a[t + s] ? a[t] : a[t + s]; z[t] = z[t] > z[t + s] ? z[t] : z[t + s]; } __syncthreads(); }
  const unsigned long long now = __builtin_amdgcn_s_memrealtime();
  unsigned long long d = (z[0] - a[0]) * MULT;
  if (d > 200000) d = 200000;
  const unsigned long long e = now + d;
  while (__builtin_amdgcn_s_memrealtime() < e) __builtin_amdgcn_s_sleep(1);
}

constexpr int LDS1 = 96 * 1024 + 16 * 1024 + 4096;

__global__ __launch_bounds__(256, 1) void k_attn(const uint16_t* __restrict__ x, const uint16_t* __restrict__ kc,
                                                 const uint16_t* __restrict__ vc, const uint16_t* __restrict__ wq,
                                                 uint16_t* __restrict__ ows) {
  constexpr int STAMP_BASE = 0;
  STAMP_AT(0);
  extern __shared__ __attribute__((aligned(16))) char smem[];
  LAS char* L = (LAS char*)smem;
  const int bid = blockIdx.x;
  const int h = 4 * (bid & 7) + ((bid >> 3) & 3);
  const int b = bid >> 5;
  const int kvh = h >> 1;
  const int w = __builtin_amdgcn_readfirstlane(threadIdx.x >> 6);
  const int lane = threadIdx.x & 63, c = lane & 15, g = lane >> 4;

  const __amdgpu_buffer_rsrc_t rx = __builtin_amdgcn_make_buffer_rsrc((void*)(x + (size_t)b * SQ * D_), 0, SQ * D_ * 2, RSRC_W3);
  const __amdgpu_buffer_rsrc_t rw = __builtin_amdgcn_make_buffer_rsrc((void*)(wq + h * DH), 0, (D_ * HQ * DH - h * DH) * 2, RSRC_W3);
  const __amdgpu_buffer_rsrc_t rk = __builtin_amdgcn_make_buffer_rsrc((void*)(kc + ((size_t)(b * HKV + kvh) * SK + 128 * w) * DH), 0, 128 * DH * 2, RSRC_W3);
  const __amdgpu_buffer_rsrc_t rv = __builtin_amdgcn_make_buffer_rsrc((void*)(vc + ((size_t)(b * HKV + kvh) * SK + 128 * w) * DH), 0, 128 * DH * 2, RSRC_W3);

  const int vo_w = g * 8 * 8192 + c * 16;
  const int vo_x = c * 1152 + g * 16;
  const int vo_k = c * 256 + g * 16;
  const int vo_v = g * 1024 + c * 16;

  v4u wr[3][8], xf[3][4];
#define LOADQ(i, slot) do { const int s_ = w + 4 * (i); const int sx_ = s_ < 18 ? s_ : 0; \
    _Pragma("unroll") for (int j = 0; j < 8; ++j) wr[slot][j] = ld128(rw, vo_w, (s_ * 32 + j) * 8192); \
    _Pragma("unroll") for (int t = 0; t < 4; ++t) xf[slot][t] = ld128(rx, vo_x, t * 16 * 1152 + sx_ * 64); SBAR(); } while (0)
#define QSTEP(slot) do { _Pragma("unroll") for (int e = 0; e < 8; ++e) { const v4u a = tr_col(wr[slot], e); \
    _Pragma("unroll") for (int t = 0; t < 4; ++t) acc[e][t] = mfma(a, xf[slot][t], acc[e][t]); } SBAR(); } while (0)
  LOADQ(0, 0); LOADQ(1, 1); LOADQ(2, 2);
  v4u kf[8][4];
#pragma unroll
  for (int kt = 0; kt < 4; ++kt)
#pragma unroll
    for (int s = 0; s < 4; ++s) kf[kt][s] = ld128(rk, vo_k + s * 64, kt * 4096);
  SBAR();

  f32x4 acc[8][4];
#pragma unroll
  for (int e = 0; e < 8; ++e)
#pragma unroll
    for (int t = 0; t < 4; ++t) acc[e][t] = f32x4{0, 0, 0, 0};
  QSTEP(0); LOADQ(3, 0);
  QSTEP(1); LOADQ(4, 1);
#pragma unroll
  for (int kt = 4; kt < 8; ++kt)
#pragma unroll
    for (int s = 0; s < 4; ++s) kf[kt][s] = ld128(rk, vo_k + s * 64, kt * 4096);
  SBAR();
  QSTEP(2); QSTEP(0); QSTEP(1);
  v4u vr[4][8];
#pragma unroll
  for (int u = 0; u < 4; ++u)
#pragma unroll
    for (int j = 0; j < 8; ++j) vr[u][j] = ld128(rv, vo_v + (j & 3) * 256, u * 8192 + (j >> 2) * 4096);
  SBAR();
  STAMP_AT(1);

  LAS f32x4* red = (LAS f32x4*)L;
#pragma unroll
  for (int t = 0; t < 4; ++t) {
    if (t != w) {
      const int slot = (w - t - 1) & 3;
#pragma unroll
      for (int e = 0; e < 8; ++e) red[((t * 3 + slot) * 8 + e) * 64 + lane] = acc[e][t];
    }
  }
  __syncthreads();
  f32x4 own[8];
#pragma unroll
  for (int e = 0; e < 8; ++e) own[e] = w == 0 ? acc[e][0] : w == 1 ? acc[e][1] : w == 2 ? acc[e][2] : acc[e][3];
#pragma unroll
  for (int sl = 0; sl < 3; ++sl)
#pragma unroll
    for (int e = 0; e < 8; ++e) own[e] += red[((w * 3 + sl) * 8 + e) * 64 + lane];
  LAS v4u* qf = (LAS v4u*)(L + 96 * 1024);
#pragma unroll
  for (int i = 0; i < 4; ++i) {
    v4u p = {pk2(own[0][i], own[1][i]), pk2(own[2][i], own[3][i]), pk2(own[4][i], own[5][i]), pk2(own[6][i], own[7][i])};
    qf[(w * 4 + g) * 64 + i * 16 + c] = p;
  }
  __syncthreads();
  v4u q[4][4];
#pragma unroll
  for (int t = 0; t < 4; ++t)
#pragma unroll
    for (int s = 0; s < 4; ++s) q[t][s] = qf[(t * 4 + s) * 64 + lane];
  STAMP_AT(2);

  f32x4 sc[8][4];
#pragma unroll
  for (int kt = 0; kt < 8; ++kt)
#pragma unroll
    for (int t = 0; t < 4; ++t) {
      f32x4 a = {0, 0, 0, 0};
#pragma unroll
      for (int s = 0; s < 4; ++s) a = mfma(kf[kt][s], q[t][s], a);
      sc[kt][t] = a;
    }
  constexpr float CS = 0.088388347648318447f * 1.4426950408889634f;
  float mx[4], ls[4];
  v4u pf[4][4];
#pragma unroll
  for (int t = 0; t < 4; ++t) {
    float m = sc[0][t][0];
#pragma unroll
    for (int kt = 0; kt < 8; ++kt)
#pragma unroll
      for (int i = 0; i < 4; ++i) m = fmaxf(m, sc[kt][t][i]);
    m = fmaxf(m, __shfl_xor(m, 16));
    m = fmaxf(m, __shfl_xor(m, 32));
    mx[t] = m * CS;
    float l = 0.f;
#pragma unroll
    for (int kt = 0; kt < 8; ++kt)
#pragma unroll
      for (int i = 0; i < 4; ++i) {
        const float p = __builtin_amdgcn_exp2f(fmaf(sc[kt][t][i], CS, -mx[t]));
        sc[kt][t][i] = p;
        l += p;
      }
    l += __shfl_xor(l, 16);
    l += __shfl_xor(l, 32);
    ls[t] = l;
#pragma unroll
    for (int u = 0; u < 4; ++u)
      pf[u][t] = v4u{pk2(sc[2 * u][t][0], sc[2 * u][t][1]), pk2(sc[2 * u][t][2], sc[2 * u][t][3]),
                     pk2(sc[2 * u + 1][t][0], sc[2 * u + 1][t][1]), pk2(sc[2 * u + 1][t][2], sc[2 * u + 1][t][3])};
  }
  LAS float* ml = (LAS float*)(L + 112 * 1024);
  if (g == 0) {
#pragma unroll
    for (int t = 0; t < 4; ++t) {
      ml[(w * 4 + t) * 16 + c] = mx[t];
      ml[256 + (w * 4 + t) * 16 + c] = ls[t];
    }
  }
  STAMP_AT(3);
#pragma unroll
  for (int e = 0; e < 8; ++e)
#pragma unroll
    for (int t = 0; t < 4; ++t) acc[e][t] = f32x4{0, 0, 0, 0};
#pragma unroll
  for (int u = 0; u < 4; ++u) {
#pragma unroll
    for (int e = 0; e < 8; ++e) {
      const v4u a = tr_col(vr[u], e);
#pragma unroll
      for (int t = 0; t < 4; ++t) acc[e][t] = mfma(a, pf[u][t], acc[e][t]);
    }
  }
  STAMP_AT(4);
#pragma unroll
  for (int t = 0; t < 4; ++t) {
    if (t != w) {
      const int slot = (w - t - 1) & 3;
#pragma unroll
      for (int e = 0; e < 8; ++e) red[((t * 3 + slot) * 8 + e) * 64 + lane] = acc[e][t];
    }
  }
  __syncthreads();
  float mw[4], lw[4];
#pragma unroll
  for (int ww = 0; ww < 4; ++ww) {
    const int src = (w + 1 + ww) & 3;
    mw[ww] = ml[(src * 4 + w) * 16 + c];
    lw[ww] = ml[256 + (src * 4 + w) * 16 + c];
  }
  const float M = fmaxf(fmaxf(mw[0], mw[1]), fmaxf(mw[2], mw[3]));
  float al[4], Ls = 0.f;
#pragma unroll
  for (int ww = 0; ww < 4; ++ww) {
    al[ww] = __builtin_amdgcn_exp2f(mw[ww] - M);
    Ls += al[ww] * lw[ww];
  }
  const float inv = 1.0f / Ls;
#pragma unroll
  for (int ww = 0; ww < 4; ++ww) al[ww] *= inv;
#pragma unroll
  for (int e = 0; e < 8; ++e) own[e] = (w == 0 ? acc[e][0] : w == 1 ? acc[e][1] : w == 2 ? acc[e][2] : acc[e][3]) * al[3];
#pragma unroll
  for (int sl = 0; sl < 3; ++sl)
#pragma unroll
    for (int e = 0; e < 8; ++e) own[e] += red[((w * 3 + sl) * 8 + e) * 64 + lane] * al[sl];
  uint16_t* orow = ows + (size_t)(b * SQ + 16 * w + c) * (HQ * DH) + h * DH + 32 * g;
#pragma unroll
  for (int i = 0; i < 4; ++i) {
    v4u p = {pk2(own[0][i], own[1][i]), pk2(own[2][i], own[3][i]), pk2(own[4][i], own[5][i]), pk2(own[6][i], own[7][i])};
    *(v4u*)(orow + 8 * i) = p;
  }
  STAMP_AT(5);
}

__global__ __launch_bounds__(512, 1) void k_oproj(const uint16_t* __restrict__ ows, const uint16_t* __restrict__ wo, uint16_t* __restrict__ out) {
  constexpr int STAMP_BASE = 256;
  STAMP_AT(0);
  __shared__ f32x4 red[8 * 8 * 64];
  const int bid = blockIdx.x;
  const int mt = bid / 9, nt = bid % 9;
  const int w = __builtin_amdgcn_readfirstlane(threadIdx.x >> 6);
  const int lane = threadIdx.x & 63, c = lane & 15, g = lane >> 4;
  const __amdgpu_buffer_rsrc_t ro = __builtin_amdgcn_make_buffer_rsrc((void*)(ows + (size_t)mt * 32 * 4096 + w * 512), 0, 32 * 4096 * 2, RSRC_W3);
  const __amdgpu_buffer_rsrc_t rw = __builtin_amdgcn_make_buffer_rsrc((void*)(wo + (size_t)w * 512 * 576 + nt * 64), 0, 512 * 576 * 2, RSRC_W3);
  const int vo_o = c * 8192 + g * 16;
  const int vo_w = g * 8 * 1152 + c * 8;
  f32x4 acc[4][2];
#pragma unroll
  for (int e = 0; e < 4; ++e) acc[e][0] = acc[e][1] = f32x4{0, 0, 0, 0};
#pragma unroll 4
  for (int s = 0; s < 16; ++s) {
    v2u r[8];
#pragma unroll
    for (int j = 0; j < 8; ++j) r[j] = __builtin_amdgcn_raw_buffer_load_b64(rw, vo_w, (s * 32 + j) * 1152, 0);
    const v4u o0 = ld128(ro, vo_o, s * 64), o1 = ld128(ro, vo_o, 16 * 8192 + s * 64);
#pragma unroll
    for (int e = 0; e < 4; ++e) {
      const unsigned sel = (e & 1) ? 0x07060302u : 0x05040100u;
      v4u a;
      a.x = __builtin_amdgcn_perm(r[1][e >> 1], r[0][e >> 1], sel);
      a.y = __builtin_amdgcn_perm(r[3][e >> 1], r[2][e >> 1], sel);
      a.z = __builtin_amdgcn_perm(r[5][e >> 1], r[4][e >> 1], sel);
      a.w = __builtin_amdgcn_perm(r[7][e >> 1], r[6][e >> 1], sel);
      acc[e][0] = mfma(a, o0, acc[e][0]);
      acc[e][1] = mfma(a, o1, acc[e][1]);
    }
  }
#pragma unroll
  for (int e = 0; e < 4; ++e)
#pragma unroll
    for (int t = 0; t < 2; ++t) red[(w * 8 + e * 2 + t) * 64 + lane] = acc[e][t];
  __syncthreads();
  if (w < 2) {
    f32x4 v[4];
#pragma unroll
    for (int e = 0; e < 4; ++e) {
      v[e] = f32x4{0, 0, 0, 0};
#pragma unroll
      for (int ww = 0; ww < 8; ++ww) v[e] += red[(ww * 8 + e * 2 + w) * 64 + lane];
    }
    uint16_t* orow = out + (size_t)(mt * 32 + w * 16 + c) * 576 + nt * 64 + 16 * g;
    v4u p0 = {pk2(v[0][0], v[1][0]), pk2(v[2][0], v[3][0]), pk2(v[0][1], v[1][1]), pk2(v[2][1], v[3][1])};
    v4u p1 = {pk2(v[0][2], v[1][2]), pk2(v[2][2], v[3][2]), pk2(v[0][3], v[1][3]), pk2(v[2][3], v[3][3])};
    *(v4u*)(orow) = p0;
    *(v4u*)(orow + 8) = p1;
  }
  STAMP_AT(5);
}

extern "C" void kernel_launch(void* const* d_in, const int* in_sizes, int n_in, void* d_out, int out_size, void* d_ws,
                              size_t ws_size, hipStream_t stream) {
  const uint16_t* x = (const uint16_t*)d_in[0];
  const uint16_t* kc = (const uint16_t*)d_in[1];
  const uint16_t* vc = (const uint16_t*)d_in[2];
  const uint16_t* wq = (const uint16_t*)d_in[3];
  const uint16_t* wo = (const uint16_t*)d_in[4];
  uint16_t* ows = (uint16_t*)d_ws;
  (void)hipFuncSetAttribute((const void*)k_attn, hipFuncAttributeMaxDynamicSharedMemorySize, LDS1);
  k_stream<<<256, 256, 0, stream>>>((const char*)PA, (const char*)PB, (unsigned long long*)((char*)d_ws + (64 << 20)), PW);
  k_attn<<<256, 256, LDS1, stream>>>(x, kc, vc, wq, ows);
  k_oproj<<<144, 512, 0, stream>>>(ows, wo, (uint16_t*)d_out);
  k_probe<<<1, 256, 0, stream>>>((const unsigned long long*)((char*)d_ws + (64 << 20)));
}
```

```cpp
#include <hip/hip_runtime.h>

constexpr int LDS_BYTES = 158208;

__global__ __attribute__((aligned(4096))) __launch_bounds__(512, 2) void k_fused(const void* x, const void* kc, const void* vc, const void* wq, const void* wo,
                                                  void* out, void* ws, void* pad) {
  asm volatile(
      "v_readfirstlane_b32 s22, %2\n"
      "s_cmp_lt_u32 s22, 192\n"
      "s_cbranch_scc1 .Lgq_args\n"
      "v_lshlrev_b32 v2, 3, %2\n"
      "v_add_u32 v2, 154112, v2\n"
      "v_mov_b32 v3, v2\n"
      "ds_write_b64 v2, v[2:3]\n"
      "s_waitcnt lgkmcnt(0)\n"
      ".Lgq_args:\n"
      "s_load_dwordx16 s[4:19], %0, 0x0\n"
      "s_getpc_b64 s[28:29]\n"
      ".Lgq_pc:\n"
      "v_lshlrev_b32 v255, 7, %2\n"
      "s_and_b32 s29, s29, 0xffff\n"
      "s_mov_b32 s30, .Lgq_code_end-.Lgq_pc\n"
      "s_mov_b32 s31, 0x20000\n"
      "s_bfe_u32 s67, %1, 0x20003\n"
      "s_lshl_b32 s26, s67, 4\n"
      "s_lshl_b64 exec, 0xffff, s26\n"
      "buffer_load_dword v255, v255, s[28:31], 0 offen\n"
      "s_mov_b64 exec, -1\n"
      "v_and_b32 v1, 63, %2\n"
      "s_and_b32 s66, %1, 7\n"
      "s_lshr_b32 s20, %1, 5\n"
      "s_lshr_b32 s22, s22, 6\n"
      "s_mov_b32 s23, 0x400000\n"
      "s_cmp_lt_u32 s22, 2\n"
      "s_cbranch_scc1 .Lgq_loader\n"
      "s_barrier\n"
      "s_branch .Lgq_compute\n"
      ".Lgq_loader:\n"
      "s_lshl2_add_u32 s56, s22, 155648\n"
      "v_mov_b32 v31, 0x7ffffff0\n"
      "v_lshrrev_b32 v2, 3, v1\n"
      "v_lshrrev_b32 v34, 1, v1\n"
      "v_lshrrev_b32 v35, 4, v1\n"
      "v_bitop3_b32 v34, v34, v35, 3 bitop3:0x28\n"
      "v_and_b32 v35, 1, v1\n"
      "v_lshlrev_b32 v35, 4, v35\n"
      "v_lshl_or_b32 v3, v34, 5, v35\n"
      "v_and_b32 v34, 2, v2\n"
      "v_lshrrev_b32 v36, 5, v1\n"
      "v_and_b32 v35, 7, v1\n"
      "v_bitop3_b32 v34, v34, v35, v36 bitop3:0x36\n"
      "v_lshlrev_b32 v34, 4, v34\n"
      "v_xor_b32 v35, 64, v34\n"
      "v_mul_u32_u24 v37, 1152, v2\n"
      "v_add_u32 v4, v37, v34\n"
      "v_add_u32 v5, v37, v35\n"
      "v_add_u32 v6, 16384, v4\n"
      "v_add_u32 v7, 24576, v5\n"
      "v_add_u32 v5, 8192, v5\n"
      "v_and_b32 v37, 3, v2\n"
      "v_lshl_or_b32 v37, v36, 4, v37\n"
      "v_lshl_or_b32 v8, v37, 13, v3\n"
      "v_add_u32 v9, 64512, v8\n"
      "v_add_u32 v10, 30720, v8\n"
      "v_add_u32 v11, 95232, v8\n"
      "s_lshl2_add_u32 s21, s66, s67\n"
      "s_mul_i32 s26, s20, 73728\n"
      "s_lshl_b32 s27, s21, 8\n"
      "s_mov_b32 s34, 73728\n"
      "s_mov_b32 s35, 0x20000\n"
      "s_sub_u32 s38, 4718592, s27\n"
      "s_mov_b32 s39, 0x20000\n"
      "s_add_u32 s57, s67, s20\n"
      "s_mul_i32 s57, s57, 3\n"
      "s_lshr_b32 s57, 0xc6633198, s57\n"
      "s_and_b32 s57, s57, 7\n"
      "s_mul_i32 s52, s22, 36864\n"
      "s_lshl_b32 s55, s57, 7\n"
      "s_add_u32 s52, s52, s55\n"
      "s_lshl_b32 s53, s57, 19\n"
      "s_lshl_b32 s58, s22, 7\n"
      "s_add_u32 s53, s53, s58\n"
      "s_lshl_b32 s55, s22, 12\n"
      "s_barrier\n"
      "s_waitcnt lgkmcnt(0)\n"
      "s_load_dword s65, s[10:11], s53\n"
      "s_add_u32 s32, s4, s26\n"
      "s_addc_u32 s33, s5, 0\n"
      "s_and_b32 s33, s33, 0xffff\n"
      "s_add_u32 s36, s10, s27\n"
      "s_addc_u32 s37, s11, 0\n"
      "s_and_b32 s37, s37, 0xffff\n"
      "s_add_u32 m0, s55, 139264\n"
      "s_nop 0\n"
      "buffer_load_dwordx4 v4, s[32:35], s52 offen lds\n"
      "buffer_load_dwordx4 v5, s[32:35], s52 offen offset:1024 lds\n"
      "buffer_load_dwordx4 v6, s[32:35], s52 offen offset:2048 lds\n"
      "buffer_load_dwordx4 v7, s[32:35], s52 offen offset:3072 lds\n"
      "s_add_u32 s52, s52, 128\n"
      "s_add_u32 m0, s55, 147456\n"
      "s_nop 0\n"
      "buffer_load_dwordx4 v8, s[36:39], s53 offen lds\n"
      "buffer_load_dwordx4 v9, s[36:39], s53 offen offset:1024 lds\n"
      "buffer_load_dwordx4 v10, s[36:39], s53 offen offset:2048 lds\n"
      "buffer_load_dwordx4 v11, s[36:39], s53 offen offset:3072 lds\n"
      "s_add_u32 s53, s53, 262144\n"
      "s_add_u32 m0, s56, 0\n"
      "s_mov_b64 exec, 1\n"
      "buffer_load_dword v31, s[32:35], 0 offen lds\n"
      "s_mov_b64 exec, -1\n"
      "s_add_u32 m0, s55, 0\n"
      "s_nop 0\n"
      "buffer_load_dwordx4 v8, s[36:39], s53 offen lds\n"
      "buffer_load_dwordx4 v9, s[36:39], s53 offen offset:1024 lds\n"
      "buffer_load_dwordx4 v10, s[36:39], s53 offen offset:2048 lds\n"
      "buffer_load_dwordx4 v11, s[36:39], s53 offen offset:3072 lds\n"
      "s_add_u32 s53, s53, 262144\n"
      "s_add_u32 m0, s56, 8\n"
      "s_mov_b64 exec, 1\n"
      "buffer_load_dword v31, s[32:35], 0 offen lds\n"
      "s_mov_b64 exec, -1\n"
      "s_mul_i32 s26, s20, 2134144\n"
      "s_load_dword s65, s[16:17], s26\n"
      "s_add_u32 m0, s55, 8192\n"
      "s_waitcnt vmcnt(22)\n"
      "buffer_load_dwordx4 v4, s[32:35], s52 offen lds\n"
      "buffer_load_dwordx4 v5, s[32:35], s52 offen offset:1024 lds\n"
      "buffer_load_dwordx4 v6, s[32:35], s52 offen offset:2048 lds\n"
      "buffer_load_dwordx4 v7, s[32:35], s52 offen offset:3072 lds\n"
      "s_add_u32 s52, s52, 128\n"
      "s_add_u32 m0, s55, 16384\n"
      "s_waitcnt vmcnt(22)\n"
      "buffer_load_dwordx4 v8, s[36:39], s53 offen lds\n"
      "buffer_load_dwordx4 v9, s[36:39], s53 offen offset:1024 lds\n"
      "buffer_load_dwordx4 v10, s[36:39], s53 offen offset:2048 lds\n"
      "buffer_load_dwordx4 v11, s[36:39], s53 offen offset:3072 lds\n"
      "s_add_u32 s53, s53, 262144\n"
      "s_add_u32 m0, s55, 24576\n"
      "s_waitcnt vmcnt(22)\n"
      "buffer_load_dwordx4 v8, s[36:39], s53 offen lds\n"
      "buffer_load_dwordx4 v9, s[36:39], s53 offen offset:1024 lds\n"
      "buffer_load_dwordx4 v10, s[36:39], s53 offen offset:2048 lds\n"
      "buffer_load_dwordx4 v11, s[36:39], s53 offen offset:3072 lds\n"
      "s_add_u32 s53, s53, 262144\n"
      "s_add_u32 m0, s56, 16\n"
      "s_mov_b64 exec, 1\n"
      "buffer_load_dword v31, s[32:35], 0 offen lds\n"
      "s_mov_b64 exec, -1\n"
      "v_lshlrev_b32 v32, 2, v1\n"
      "v_add_u32 v32, 156160, v32\n"
      "s_mov_b32 s54, 0\n"
      "v_lshl_or_b32 v12, v2, 8, v34\n"
      "v_lshl_or_b32 v13, v2, 8, v35\n"
      "v_add_u32 v14, 63488, v12\n"
      "v_add_u32 v15, 95232, v13\n"
      "v_add_u32 v13, 31744, v13\n"
      "v_lshl_or_b32 v16, v2, 8, v3\n"
      "v_add_u32 v17, 31744, v16\n"
      "v_add_u32 v18, 63488, v16\n"
      "v_add_u32 v19, 95232, v16\n"
      "s_lshr_b32 s27, s67, 1\n"
      "s_lshl1_add_u32 s27, s20, s27\n"
      "s_and_b32 s26, s67, 1\n"
      "s_mul_i32 s26, s26, 1\n"
      "s_xor_b32 s27, s27, s26\n"
      "s_lshl_b32 s27, s27, 11\n"
      "s_or_b32 s58, s58, s27\n"
      "s_lshr_b32 s27, s21, 1\n"
      "s_lshl4_add_u32 s26, s20, s27\n"
      "s_lshl_b32 s26, s26, 17\n"
      "s_add_u32 s40, s6, s26\n"
      "s_addc_u32 s41, s7, 0\n"
      "s_and_b32 s41, s41, 0xffff\n"
      "s_mov_b32 s42, 131072\n"
      "s_mov_b32 s43, 0x20000\n"
      "s_add_u32 s44, s8, s26\n"
      "s_addc_u32 s45, s9, 0\n"
      "s_and_b32 s45, s45, 0xffff\n"
      "s_mov_b32 s46, 131072\n"
      "s_mov_b32 s47, 0x20000\n"
      "s_cmp_eq_u32 s22, 0\n"
      "s_cselect_b64 s[60:61], s[6:7], s[8:9]\n"
      "s_load_dword s65, s[60:61], s26\n"
      "s_lshl3_add_u32 s26, s20, s66\n"
      "s_lshl_b32 s26, s26, 5\n"
      "s_add_u32 s26, s26, s67\n"
      "s_mul_i32 s26, s26, 288\n"
      "s_mul_i32 s27, s20, 2134144\n"
      "s_add_u32 s27, s27, 1249280\n"
      "s_cmp_eq_u32 s22, 0\n"
      "s_cselect_b64 s[60:61], s[14:15], s[16:17]\n"
      "s_cselect_b32 s26, s26, s27\n"
      "s_load_dword s65, s[60:61], s26\n"
      "s_mul_i32 s27, s22, 2097148\n"
      "s_add_u32 s27, s27, 2621440\n"
      "s_load_dword s65, s[10:11], s27\n"
      "s_mul_i32 s26, s66, 589824\n"
      "s_mul_i32 s27, s22, 589820\n"
      "s_add_u32 s27, s26, s27\n"
      "s_load_dword s65, s[12:13], s27\n"
      "s_mul_i32 s27, s67, 288\n"
      "s_add_u32 s26, s26, s27\n"
      "s_add_u32 s48, s12, s26\n"
      "s_addc_u32 s49, s13, 0\n"
      "s_and_b32 s49, s49, 0xffff\n"
      "s_sub_u32 s50, 589824, s27\n"
      "s_mov_b32 s51, 0x20000\n"
      "s_movk_i32 s26, 3641\n"
      "s_movk_i32 s27, 864\n"
      "v_add_u32 v34, 0, v1\n"
      "v_mul_u32_u24 v35, s26, v34\n"
      "v_lshrrev_b32 v35, 16, v35\n"
      "v_lshlrev_b32 v34, 4, v34\n"
      "v_mad_u32_u24 v20, v35, s27, v34\n"
      "v_add_u32 v34, 64, v1\n"
      "v_mul_u32_u24 v35, s26, v34\n"
      "v_lshrrev_b32 v35, 16, v35\n"
      "v_lshlrev_b32 v34, 4, v34\n"
      "v_mad_u32_u24 v21, v35, s27, v34\n"
      "v_subrev_u32 v21, 1024, v21\n"
      "v_add_u32 v34, 128, v1\n"
      "v_mul_u32_u24 v35, s26, v34\n"
      "v_lshrrev_b32 v35, 16, v35\n"
      "v_lshlrev_b32 v34, 4, v34\n"
      "v_mad_u32_u24 v22, v35, s27, v34\n"
      "v_subrev_u32 v22, 2048, v22\n"
      "v_add_u32 v34, 192, v1\n"
      "v_mul_u32_u24 v35, s26, v34\n"
      "v_lshrrev_b32 v35, 16, v35\n"
      "v_lshlrev_b32 v34, 4, v34\n"
      "v_mad_u32_u24 v23, v35, s27, v34\n"
      "v_subrev_u32 v23, 3072, v23\n"
      "v_add_u32 v34, 256, v1\n"
      "v_mul_u32_u24 v35, s26, v34\n"
      "v_lshrrev_b32 v35, 16, v35\n"
      "v_lshlrev_b32 v34, 4, v34\n"
      "v_mad_u32_u24 v24, v35, s27, v34\n"
      "v_add_u32 v34, 320, v1\n"
      "v_mul_u32_u24 v35, s26, v34\n"
      "v_lshrrev_b32 v35, 16, v35\n"
      "v_lshlrev_b32 v34, 4, v34\n"
      "v_mad_u32_u24 v25, v35, s27, v34\n"
      "v_subrev_u32 v25, 1024, v25\n"
      "v_add_u32 v34, 384, v1\n"
      "v_mul_u32_u24 v35, s26, v34\n"
      "v_lshrrev_b32 v35, 16, v35\n"
      "v_lshlrev_b32 v34, 4, v34\n"
      "v_mad_u32_u24 v26, v35, s27, v34\n"
      "v_subrev_u32 v26, 2048, v26\n"
      "v_add_u32 v34, 448, v1\n"
      "v_mul_u32_u24 v35, s26, v34\n"
      "v_lshrrev_b32 v35, 16, v35\n"
      "v_lshlrev_b32 v34, 4, v34\n"
      "v_mad_u32_u24 v27, v35, s27, v34\n"
      "v_subrev_u32 v27, 3072, v27\n"
      "v_add_u32 v34, 512, v1\n"
      "v_mul_u32_u24 v35, s26, v34\n"
      "v_lshrrev_b32 v35, 16, v35\n"
      "v_lshlrev_b32 v34, 4, v34\n"
      "v_mad_u32_u24 v28, v35, s27, v34\n"
      "s_lshl_b32 s59, s67, 2\n"
      ".Lgq_pad_1:\n"
      ".Lgq_pad267:\n"
      "s_cbranch_execz .Lgq_pad313\n"
      "s_add_u32 m0, s55, 32768\n"
      "s_waitcnt vmcnt(22)\n"
      "buffer_load_dwordx4 v4, s[32:35], s52 offen lds\n"
      "buffer_load_dwordx4 v5, s[32:35], s52 offen offset:1024 lds\n"
      "buffer_load_dwordx4 v6, s[32:35], s52 offen offset:2048 lds\n"
      "buffer_load_dwordx4 v7, s[32:35], s52 offen offset:3072 lds\n"
      "s_add_u32 s52, s52, 128\n"
      "s_add_u32 m0, s55, 40960\n"
      "s_waitcnt vmcnt(22)\n"
      "buffer_load_dwordx4 v8, s[36:39], s53 offen lds\n"
      "buffer_load_dwordx4 v9, s[36:39], s53 offen offset:1024 lds\n"
      "buffer_load_dwordx4 v10, s[36:39], s53 offen offset:2048 lds\n"
      "buffer_load_dwordx4 v11, s[36:39], s53 offen offset:3072 lds\n"
      "s_add_u32 s53, s53, 262144\n"
      "s_add_u32 m0, s55, 49152\n"
      "s_waitcnt vmcnt(22)\n"
      "buffer_load_dwordx4 v8, s[36:39], s53 offen lds\n"
      "buffer_load_dwordx4 v9, s[36:39], s53 offen offset:1024 lds\n"
      "buffer_load_dwordx4 v10, s[36:39], s53 offen offset:2048 lds\n"
      "buffer_load_dwordx4 v11, s[36:39], s53 offen offset:3072 lds\n"
      "s_add_u32 s53, s53, 262144\n"
      "s_add_u32 m0, s56, 24\n"
      "s_mov_b64 exec, 1\n"
      "buffer_load_dword v31, s[32:35], 0 offen lds\n"
      "s_mov_b64 exec, -1\n"
      "s_cmp_eq_u32 s57, 6\n"
      "s_cselect_b32 s26, 1152, 0\n"
      "s_cselect_b32 s27, 4718592, 0\n"
      "s_sub_u32 s52, s52, s26\n"
      "s_sub_u32 s53, s53, s27\n"
      "s_add_u32 m0, s55, 57344\n"
      "s_waitcnt vmcnt(22)\n"
      "buffer_load_dwordx4 v4, s[32:35], s52 offen lds\n"
      "buffer_load_dwordx4 v5, s[32:35], s52 offen offset:1024 lds\n"
      "buffer_load_dwordx4 v6, s[32:35], s52 offen offset:2048 lds\n"
      "buffer_load_dwordx4 v7, s[32:35], s52 offen offset:3072 lds\n"
      "s_add_u32 s52, s52, 128\n"
      "s_add_u32 m0, s55, 65536\n"
      "s_waitcnt vmcnt(22)\n"
      "buffer_load_dwordx4 v8, s[36:39], s53 offen lds\n"
      "buffer_load_dwordx4 v9, s[36:39], s53 offen offset:1024 lds\n"
      "buffer_load_dwordx4 v10, s[36:39], s53 offen offset:2048 lds\n"
      "buffer_load_dwordx4 v11, s[36:39], s53 offen offset:3072 lds\n"
      ".Lgq_pad313:\n"
      "s_cbranch_execz .Lgq_pad360\n"
      "s_add_u32 s53, s53, 262144\n"
      "s_add_u32 m0, s55, 73728\n"
      "s_waitcnt vmcnt(22)\n"
      "buffer_load_dwordx4 v8, s[36:39], s53 offen lds\n"
      "buffer_load_dwordx4 v9, s[36:39], s53 offen offset:1024 lds\n"
      "buffer_load_dwordx4 v10, s[36:39], s53 offen offset:2048 lds\n"
      "buffer_load_dwordx4 v11, s[36:39], s53 offen offset:3072 lds\n"
      "s_add_u32 s53, s53, 262144\n"
      "s_add_u32 m0, s56, 32\n"
      "s_mov_b64 exec, 1\n"
      "buffer_load_dword v31, s[32:35], 0 offen lds\n"
      "s_mov_b64 exec, -1\n"
      "s_add_u32 m0, s55, 81920\n"
      "s_waitcnt vmcnt(22)\n"
      "buffer_load_dwordx4 v4, s[32:35], s52 offen lds\n"
      "buffer_load_dwordx4 v5, s[32:35], s52 offen offset:1024 lds\n"
      "buffer_load_dwordx4 v6, s[32:35], s52 offen offset:2048 lds\n"
      "buffer_load_dwordx4 v7, s[32:35], s52 offen offset:3072 lds\n"
      "s_add_u32 s52, s52, 128\n"
      "s_add_u32 m0, s55, 90112\n"
      "s_waitcnt vmcnt(22)\n"
      "buffer_load_dwordx4 v8, s[36:39], s53 offen lds\n"
      "buffer_load_dwordx4 v9, s[36:39], s53 offen offset:1024 lds\n"
      "buffer_load_dwordx4 v10, s[36:39], s53 offen offset:2048 lds\n"
      "buffer_load_dwordx4 v11, s[36:39], s53 offen offset:3072 lds\n"
      "s_add_u32 s53, s53, 262144\n"
      "s_add_u32 m0, s55, 98304\n"
      "s_waitcnt vmcnt(22)\n"
      "buffer_load_dwordx4 v8, s[36:39], s53 offen lds\n"
      "buffer_load_dwordx4 v9, s[36:39], s53 offen offset:1024 lds\n"
      "buffer_load_dwordx4 v10, s[36:39], s53 offen offset:2048 lds\n"
      "buffer_load_dwordx4 v11, s[36:39], s53 offen offset:3072 lds\n"
      "s_add_u32 s53, s53, 262144\n"
      "s_add_u32 m0, s56, 40\n"
      "s_mov_b64 exec, 1\n"
      "buffer_load_dword v31, s[32:35], 0 offen lds\n"
      "s_mov_b64 exec, -1\n"
      "s_memrealtime s[30:31]\n"
      "s_add_u32 m0, s55, 106496\n"
      "s_waitcnt vmcnt(22)\n"
      "buffer_load_dwordx4 v4, s[32:35], s52 offen lds\n"
      "buffer_load_dwordx4 v5, s[32:35], s52 offen offset:1024 lds\n"
      "buffer_load_dwordx4 v6, s[32:35], s52 offen offset:2048 lds\n"
      "buffer_load_dwordx4 v7, s[32:35], s52 offen offset:3072 lds\n"
      ".Lgq_pad360:\n"
      "s_cbranch_execz .Lgq_pad409\n"
      "s_add_u32 s52, s52, 128\n"
      "s_add_u32 m0, s55, 114688\n"
      "s_waitcnt vmcnt(22)\n"
      "buffer_load_dwordx4 v8, s[36:39], s53 offen lds\n"
      "buffer_load_dwordx4 v9, s[36:39], s53 offen offset:1024 lds\n"
      "buffer_load_dwordx4 v10, s[36:39], s53 offen offset:2048 lds\n"
      "buffer_load_dwordx4 v11, s[36:39], s53 offen offset:3072 lds\n"
      "s_add_u32 s53, s53, 262144\n"
      "s_add_u32 m0, s55, 122880\n"
      "s_waitcnt vmcnt(22)\n"
      "buffer_load_dwordx4 v8, s[36:39], s53 offen lds\n"
      "buffer_load_dwordx4 v9, s[36:39], s53 offen offset:1024 lds\n"
      "buffer_load_dwordx4 v10, s[36:39], s53 offen offset:2048 lds\n"
      "buffer_load_dwordx4 v11, s[36:39], s53 offen offset:3072 lds\n"
      "s_add_u32 s53, s53, 262144\n"
      "s_add_u32 m0, s56, 48\n"
      "s_mov_b64 exec, 1\n"
      "buffer_load_dword v31, s[32:35], 0 offen lds\n"
      "s_mov_b64 exec, -1\n"
      "s_cmp_eq_u32 s57, 3\n"
      "s_cselect_b32 s26, 1152, 0\n"
      "s_cselect_b32 s27, 4718592, 0\n"
      "s_sub_u32 s52, s52, s26\n"
      "s_sub_u32 s53, s53, s27\n"
      "s_add_u32 m0, s55, 131072\n"
      "s_waitcnt vmcnt(22)\n"
      "buffer_load_dwordx4 v4, s[32:35], s52 offen lds\n"
      "buffer_load_dwordx4 v5, s[32:35], s52 offen offset:1024 lds\n"
      "buffer_load_dwordx4 v6, s[32:35], s52 offen offset:2048 lds\n"
      "buffer_load_dwordx4 v7, s[32:35], s52 offen offset:3072 lds\n"
      "s_add_u32 s52, s52, 128\n"
      "s_cmp_ge_i32 s54, 1\n"
      "s_cbranch_scc0 .Lgq_f1\n"
      ".Lgq_fr1:\n"
      "s_add_u32 m0, s55, 139264\n"
      "s_waitcnt vmcnt(22)\n"
      "buffer_load_dwordx4 v8, s[36:39], s53 offen lds\n"
      "buffer_load_dwordx4 v9, s[36:39], s53 offen offset:1024 lds\n"
      "buffer_load_dwordx4 v10, s[36:39], s53 offen offset:2048 lds\n"
      "buffer_load_dwordx4 v11, s[36:39], s53 offen offset:3072 lds\n"
      "s_add_u32 s53, s53, 262144\n"
      "s_add_u32 m0, s55, 147456\n"
      "s_waitcnt vmcnt(22)\n"
      "buffer_load_dwordx4 v8, s[36:39], s53 offen lds\n"
      "buffer_load_dwordx4 v9, s[36:39], s53 offen offset:1024 lds\n"
      "buffer_load_dwordx4 v10, s[36:39], s53 offen offset:2048 lds\n"
      "buffer_load_dwordx4 v11, s[36:39], s53 offen offset:3072 lds\n"
      ".Lgq_pad409:\n"
      "s_cbranch_execz .Lgq_pad462\n"
      "s_add_u32 s53, s53, 262144\n"
      "s_add_u32 m0, s56, 56\n"
      "s_mov_b64 exec, 1\n"
      "buffer_load_dword v31, s[32:35], 0 offen lds\n"
      "s_mov_b64 exec, -1\n"
      "s_add_u32 m0, s55, 0\n"
      "s_waitcnt vmcnt(22)\n"
      "buffer_load_dwordx4 v4, s[32:35], s52 offen lds\n"
      "buffer_load_dwordx4 v5, s[32:35], s52 offen offset:1024 lds\n"
      "buffer_load_dwordx4 v6, s[32:35], s52 offen offset:2048 lds\n"
      "buffer_load_dwordx4 v7, s[32:35], s52 offen offset:3072 lds\n"
      "s_add_u32 s52, s52, 128\n"
      "s_cmp_ge_i32 s54, 2\n"
      "s_cbranch_scc0 .Lgq_f2\n"
      ".Lgq_fr2:\n"
      "s_add_u32 m0, s55, 8192\n"
      "s_waitcnt vmcnt(22)\n"
      "buffer_load_dwordx4 v8, s[36:39], s53 offen lds\n"
      "buffer_load_dwordx4 v9, s[36:39], s53 offen offset:1024 lds\n"
      "buffer_load_dwordx4 v10, s[36:39], s53 offen offset:2048 lds\n"
      "buffer_load_dwordx4 v11, s[36:39], s53 offen offset:3072 lds\n"
      "s_add_u32 s53, s53, 262144\n"
      "s_add_u32 m0, s55, 16384\n"
      "s_waitcnt vmcnt(22)\n"
      "buffer_load_dwordx4 v8, s[36:39], s53 offen lds\n"
      "buffer_load_dwordx4 v9, s[36:39], s53 offen offset:1024 lds\n"
      "buffer_load_dwordx4 v10, s[36:39], s53 offen offset:2048 lds\n"
      "buffer_load_dwordx4 v11, s[36:39], s53 offen offset:3072 lds\n"
      "s_add_u32 s53, s53, 262144\n"
      "s_add_u32 m0, s56, 64\n"
      "s_mov_b64 exec, 1\n"
      "buffer_load_dword v31, s[32:35], 0 offen lds\n"
      "s_mov_b64 exec, -1\n"
      "s_memrealtime s[28:29]\n"
      "s_add_u32 m0, s55, 24576\n"
      "s_waitcnt vmcnt(22)\n"
      "buffer_load_dwordx4 v4, s[32:35], s52 offen lds\n"
      "buffer_load_dwordx4 v5, s[32:35], s52 offen offset:1024 lds\n"
      "buffer_load_dwordx4 v6, s[32:35], s52 offen offset:2048 lds\n"
      "buffer_load_dwordx4 v7, s[32:35], s52 offen offset:3072 lds\n"
      "s_add_u32 s52, s52, 128\n"
      "s_cmp_ge_i32 s54, 3\n"
      "s_cbranch_scc0 .Lgq_f3\n"
      ".Lgq_fr3:\n"
      "s_add_u32 m0, s55, 32768\n"
      "s_waitcnt vmcnt(22)\n"
      "buffer_load_dwordx4 v8, s[36:39], s53 offen lds\n"
      "buffer_load_dwordx4 v9, s[36:39], s53 offen offset:1024 lds\n"
      "buffer_load_dwordx4 v10, s[36:39], s53 offen offset:2048 lds\n"
      "buffer_load_dwordx4 v11, s[36:39], s53 offen offset:3072 lds\n"
      ".Lgq_pad462:\n"
      "s_cbranch_execz .Lgq_pad517\n"
      "s_add_u32 s53, s53, 262144\n"
      "s_add_u32 m0, s55, 40960\n"
      "s_waitcnt vmcnt(22)\n"
      "buffer_load_dwordx4 v8, s[36:39], s53 offen lds\n"
      "buffer_load_dwordx4 v9, s[36:39], s53 offen offset:1024 lds\n"
      "buffer_load_dwordx4 v10, s[36:39], s53 offen offset:2048 lds\n"
      "buffer_load_dwordx4 v11, s[36:39], s53 offen offset:3072 lds\n"
      "s_add_u32 s53, s53, 262144\n"
      "s_add_u32 m0, s56, 72\n"
      "s_mov_b64 exec, 1\n"
      "buffer_load_dword v31, s[32:35], 0 offen lds\n"
      "s_mov_b64 exec, -1\n"
      "s_cmp_eq_u32 s22, 1\n"
      "s_mov_b32 s52, 18432\n"
      "s_mov_b32 s53, 18432\n"
      "s_cbranch_scc0 .Lgq_end\n"
      "s_andn2_b32 s58, s58, 128\n"
      "s_waitcnt lgkmcnt(0)\n"
      "s_sub_u32 s28, s28, s30\n"
      ".Lgq_kmin:\n"
      "s_cmp_lt_u32 s28, 74\n"
      "s_cbranch_scc1 .Lgq_kmin1\n"
      "s_mov_b32 m0, 49152\n"
      "s_xor_b32 s26, s58, 0\n"
      "s_waitcnt vmcnt(36)\n"
      "buffer_load_dwordx4 v12, s[40:43], s26 offen lds\n"
      "buffer_load_dwordx4 v13, s[40:43], s26 offen offset:1024 lds\n"
      "buffer_load_dwordx4 v14, s[40:43], s26 offen offset:2048 lds\n"
      "buffer_load_dwordx4 v15, s[40:43], s26 offen offset:3072 lds\n"
      "s_mov_b32 m0, 53248\n"
      "s_xor_b32 s26, s58, 128\n"
      "s_waitcnt vmcnt(36)\n"
      "buffer_load_dwordx4 v12, s[40:43], s26 offen lds\n"
      "buffer_load_dwordx4 v13, s[40:43], s26 offen offset:1024 lds\n"
      "buffer_load_dwordx4 v14, s[40:43], s26 offen offset:2048 lds\n"
      "buffer_load_dwordx4 v15, s[40:43], s26 offen offset:3072 lds\n"
      "s_mov_b32 m0, 155728\n"
      "s_mov_b64 exec, 3\n"
      "buffer_load_dword v31, s[32:35], 0 offen lds\n"
      "s_mov_b64 exec, -1\n"
      "s_cmp_ge_i32 s54, 4\n"
      "s_cbranch_scc0 .Lgq_f4\n"
      ".Lgq_fr4:\n"
      "s_mov_b32 m0, 57344\n"
      "s_xor_b32 s26, s58, 2048\n"
      "s_waitcnt vmcnt(36)\n"
      "buffer_load_dwordx4 v12, s[40:43], s26 offen lds\n"
      "buffer_load_dwordx4 v13, s[40:43], s26 offen offset:1024 lds\n"
      "buffer_load_dwordx4 v14, s[40:43], s26 offen offset:2048 lds\n"
      "buffer_load_dwordx4 v15, s[40:43], s26 offen offset:3072 lds\n"
      ".Lgq_pad517:\n"
      "s_cbranch_execz .Lgq_pad3593\n"
      "s_mov_b32 m0, 61440\n"
      "s_xor_b32 s26, s58, 2176\n"
      "s_waitcnt vmcnt(36)\n"
      "buffer_load_dwordx4 v12, s[40:43], s26 offen lds\n"
      "buffer_load_dwordx4 v13, s[40:43], s26 offen offset:1024 lds\n"
      "buffer_load_dwordx4 v14, s[40:43], s26 offen offset:2048 lds\n"
      "buffer_load_dwordx4 v15, s[40:43], s26 offen offset:3072 lds\n"
      "s_mov_b32 m0, 155736\n"
      "s_mov_b64 exec, 3\n"
      "buffer_load_dword v31, s[32:35], 0 offen lds\n"
      "s_mov_b64 exec, -1\n"
      "s_mov_b32 s62, 0x98876654\n"
      "s_mov_b32 s63, 0xccbbaa\n"
      "s_mov_b32 s18, 65536\n"
      "s_mov_b32 s64, 155744\n"
      "s_mov_b32 s61, 0\n"
      ".Lgq_kv:\n"
      "s_and_b32 s19, s62, 15\n"
      "s_lshr_b64 s[62:63], s[62:63], 4\n"
      "s_cmp_ge_i32 s54, s19\n"
      "s_cbranch_scc0 .Lgq_f5\n"
      ".Lgq_fr5:\n"
      "s_mov_b32 m0, s18\n"
      "s_xor_b32 s26, s58, s61\n"
      "s_waitcnt vmcnt(36)\n"
      "buffer_load_dwordx4 v16, s[44:47], s26 offen lds\n"
      "buffer_load_dwordx4 v17, s[44:47], s26 offen offset:1024 lds\n"
      "buffer_load_dwordx4 v18, s[44:47], s26 offen offset:2048 lds\n"
      "buffer_load_dwordx4 v19, s[44:47], s26 offen offset:3072 lds\n"
      "s_add_u32 m0, m0, 4096\n"
      "s_or_b32 s26, s26, 128\n"
      "s_waitcnt vmcnt(36)\n"
      "buffer_load_dwordx4 v16, s[44:47], s26 offen lds\n"
      "buffer_load_dwordx4 v17, s[44:47], s26 offen offset:1024 lds\n"
      "buffer_load_dwordx4 v18, s[44:47], s26 offen offset:2048 lds\n"
      "buffer_load_dwordx4 v19, s[44:47], s26 offen offset:3072 lds\n"
      "s_add_u32 s18, s18, 8192\n"
      "s_cmp_eq_u32 s18, 155648\n"
      "s_cselect_b32 s18, 0, s18\n"
      "s_add_u32 s27, s61, 4096\n"
      "s_mov_b32 m0, s18\n"
      "s_xor_b32 s26, s58, s27\n"
      "s_waitcnt vmcnt(36)\n"
      "buffer_load_dwordx4 v12, s[40:43], s26 offen lds\n"
      "buffer_load_dwordx4 v13, s[40:43], s26 offen offset:1024 lds\n"
      "buffer_load_dwordx4 v14, s[40:43], s26 offen offset:2048 lds\n"
      "buffer_load_dwordx4 v15, s[40:43], s26 offen offset:3072 lds\n"
      "s_add_u32 m0, m0, 4096\n"
      "s_or_b32 s26, s26, 128\n"
      "s_waitcnt vmcnt(36)\n"
      "buffer_load_dwordx4 v12, s[40:43], s26 offen lds\n"
      "buffer_load_dwordx4 v13, s[40:43], s26 offen offset:1024 lds\n"
      "buffer_load_dwordx4 v14, s[40:43], s26 offen offset:2048 lds\n"
      "buffer_load_dwordx4 v15, s[40:43], s26 offen offset:3072 lds\n"
      ".Lgq_pad573:\n"
      "s_cbranch_execz .Lgq_pad622\n"
      "s_add_u32 s18, s18, 8192\n"
      "s_cmp_eq_u32 s18, 155648\n"
      "s_cselect_b32 s18, 0, s18\n"
      "s_mov_b32 m0, s64\n"
      "s_mov_b64 exec, 3\n"
      "buffer_load_dword v31, s[32:35], 0 offen lds\n"
      "s_mov_b64 exec, -1\n"
      "s_add_u32 s64, s64, 8\n"
      "s_add_u32 s61, s61, 2048\n"
      "s_cmp_lg_u32 s61, 28672\n"
      "s_cbranch_scc1 .Lgq_kv\n"
      "s_cmp_ge_i32 s54, 13\n"
      "s_cbranch_scc0 .Lgq_f6\n"
      ".Lgq_fr6:\n"
      "s_mov_b32 m0, 139264\n"
      "s_xor_b32 s26, s58, 28672\n"
      "s_waitcnt vmcnt(36)\n"
      "buffer_load_dwordx4 v16, s[44:47], s26 offen lds\n"
      "buffer_load_dwordx4 v17, s[44:47], s26 offen offset:1024 lds\n"
      "buffer_load_dwordx4 v18, s[44:47], s26 offen offset:2048 lds\n"
      "buffer_load_dwordx4 v19, s[44:47], s26 offen offset:3072 lds\n"
      "s_mov_b32 m0, 143360\n"
      "s_xor_b32 s26, s58, 28800\n"
      "s_waitcnt vmcnt(36)\n"
      "buffer_load_dwordx4 v16, s[44:47], s26 offen lds\n"
      "buffer_load_dwordx4 v17, s[44:47], s26 offen offset:1024 lds\n"
      "buffer_load_dwordx4 v18, s[44:47], s26 offen offset:2048 lds\n"
      "buffer_load_dwordx4 v19, s[44:47], s26 offen offset:3072 lds\n"
      "s_mov_b32 m0, 155856\n"
      "s_mov_b64 exec, 3\n"
      "buffer_load_dword v31, s[32:35], 0 offen lds\n"
      "s_mov_b64 exec, -1\n"
      "s_mov_b32 m0, 147456\n"
      "s_xor_b32 s26, s58, 30720\n"
      "s_waitcnt vmcnt(36)\n"
      "buffer_load_dwordx4 v16, s[44:47], s26 offen lds\n"
      "buffer_load_dwordx4 v17, s[44:47], s26 offen offset:1024 lds\n"
      "buffer_load_dwordx4 v18, s[44:47], s26 offen offset:2048 lds\n"
      "buffer_load_dwordx4 v19, s[44:47], s26 offen offset:3072 lds\n"
      "s_mov_b32 m0, 151552\n"
      "s_xor_b32 s26, s58, 30848\n"
      "s_waitcnt vmcnt(36)\n"
      "buffer_load_dwordx4 v16, s[44:47], s26 offen lds\n"
      "buffer_load_dwordx4 v17, s[44:47], s26 offen offset:1024 lds\n"
      "buffer_load_dwordx4 v18, s[44:47], s26 offen offset:2048 lds\n"
      "buffer_load_dwordx4 v19, s[44:47], s26 offen offset:3072 lds\n"
      ".Lgq_pad622:\n"
      "s_cbranch_execz .Lgq_pad679\n"
      "s_mov_b32 m0, 155864\n"
      "s_mov_b64 exec, 3\n"
      "buffer_load_dword v31, s[32:35], 0 offen lds\n"
      "s_mov_b64 exec, -1\n"
      "s_lshl2_add_u32 s26, s20, s67\n"
      "s_mul_i32 s26, s26, 18432\n"
      "v_lshlrev_b32 v239, 7, v1\n"
      "buffer_load_dword v255, v239, s[48:51], s26 offen\n"
      "s_add_u32 s26, s26, 8192\n"
      "buffer_load_dword v255, v239, s[48:51], s26 offen\n"
      "s_add_u32 s26, s26, 8192\n"
      "s_mov_b64 exec, 0xffff\n"
      "buffer_load_dword v255, v239, s[48:51], s26 offen\n"
      "s_mov_b64 exec, -1\n"
      "s_mov_b32 s62, 0xfeeeed\n"
      "s_mov_b32 s63, 0x0\n"
      "s_mov_b32 s18, 0\n"
      "s_mov_b32 s64, 155880\n"
      "s_mov_b32 s61, 0\n"
      ".Lgq_wo0:\n"
      "s_xor_b32 s52, s59, s61\n"
      "s_mul_i32 s52, s52, 36864\n"
      "s_and_b32 s19, s62, 15\n"
      "s_lshr_b64 s[62:63], s[62:63], 4\n"
      "s_cmp_ge_i32 s54, s19\n"
      "s_cbranch_scc0 .Lgq_f7\n"
      ".Lgq_fr7:\n"
      "s_add_u32 m0, s18, 0\n"
      "s_waitcnt vmcnt(30)\n"
      "buffer_load_dwordx4 v20, s[48:51], s52 offen lds\n"
      "buffer_load_dwordx4 v21, s[48:51], s52 offen offset:1024 lds\n"
      "buffer_load_dwordx4 v22, s[48:51], s52 offen offset:2048 lds\n"
      "buffer_load_dwordx4 v23, s[48:51], s52 offen offset:3072 lds\n"
      "s_add_u32 m0, m0, 4096\n"
      "s_waitcnt vmcnt(30)\n"
      "buffer_load_dwordx4 v24, s[48:51], s52 offen lds\n"
      "buffer_load_dwordx4 v25, s[48:51], s52 offen offset:1024 lds\n"
      "buffer_load_dwordx4 v26, s[48:51], s52 offen offset:2048 lds\n"
      "buffer_load_dwordx4 v27, s[48:51], s52 offen offset:3072 lds\n"
      "s_add_u32 m0, m0, 4096\n"
      "s_nop 0\n"
      "buffer_load_dwordx4 v28, s[48:51], s52 offen lds\n"
      "s_add_u32 s52, s52, 36864\n"
      "s_and_b32 s19, s62, 15\n"
      "s_lshr_b64 s[62:63], s[62:63], 4\n"
      "s_cmp_ge_i32 s54, s19\n"
      "s_cbranch_scc0 .Lgq_f8\n"
      ".Lgq_fr8:\n"
      "s_add_u32 m0, s18, 9216\n"
      "s_waitcnt vmcnt(30)\n"
      "buffer_load_dwordx4 v20, s[48:51], s52 offen lds\n"
      "buffer_load_dwordx4 v21, s[48:51], s52 offen offset:1024 lds\n"
      "buffer_load_dwordx4 v22, s[48:51], s52 offen offset:2048 lds\n"
      "buffer_load_dwordx4 v23, s[48:51], s52 offen offset:3072 lds\n"
      ".Lgq_pad679:\n"
      "s_cbranch_execz .Lgq_pad689\n"
      "s_add_u32 m0, m0, 4096\n"
      "s_waitcnt vmcnt(30)\n"
      "buffer_load_dwordx4 v24, s[48:51], s52 offen lds\n"
      "buffer_load_dwordx4 v25, s[48:51], s52 offen offset:1024 lds\n"
      "buffer_load_dwordx4 v26, s[48:51], s52 offen offset:2048 lds\n"
      "buffer_load_dwordx4 v27, s[48:51], s52 offen offset:3072 lds\n"
      "s_add_u32 m0, m0, 4096\n"
      "s_nop 0\n"
      "buffer_load_dwordx4 v28, s[48:51], s52 offen lds\n"
      ".Lgq_pad689:\n"
      "s_cbranch_execz .Lgq_pad736\n"
      "s_mov_b32 m0, s64\n"
      "s_mov_b64 exec, 3\n"
      "buffer_load_dword v31, s[32:35], 0 offen lds\n"
      "s_mov_b64 exec, -1\n"
      "s_add_u32 s64, s64, 16\n"
      "s_add_u32 s18, s18, 18432\n"
      "s_add_u32 s61, s61, 2\n"
      "s_cmp_lg_u32 s61, 6\n"
      "s_cbranch_scc1 .Lgq_wo0\n"
      "s_cmp_ge_i32 s54, 16\n"
      "s_cbranch_scc0 .Lgq_f9\n"
      ".Lgq_fr9:\n"
      ".Lgq_wo1:\n"
      "s_xor_b32 s52, s59, s61\n"
      "s_mul_i32 s52, s52, 36864\n"
      "s_add_u32 m0, s18, 0\n"
      "s_waitcnt vmcnt(40)\n"
      "buffer_load_dwordx4 v20, s[48:51], s52 offen lds\n"
      "buffer_load_dwordx4 v21, s[48:51], s52 offen offset:1024 lds\n"
      "buffer_load_dwordx4 v22, s[48:51], s52 offen offset:2048 lds\n"
      "buffer_load_dwordx4 v23, s[48:51], s52 offen offset:3072 lds\n"
      "s_add_u32 m0, m0, 4096\n"
      "s_waitcnt vmcnt(40)\n"
      "buffer_load_dwordx4 v24, s[48:51], s52 offen lds\n"
      "buffer_load_dwordx4 v25, s[48:51], s52 offen offset:1024 lds\n"
      "buffer_load_dwordx4 v26, s[48:51], s52 offen offset:2048 lds\n"
      "buffer_load_dwordx4 v27, s[48:51], s52 offen offset:3072 lds\n"
      "s_add_u32 m0, m0, 4096\n"
      "s_nop 0\n"
      "buffer_load_dwordx4 v28, s[48:51], s52 offen lds\n"
      "s_add_u32 s52, s52, 36864\n"
      "s_add_u32 m0, s18, 9216\n"
      "s_waitcnt vmcnt(40)\n"
      "buffer_load_dwordx4 v20, s[48:51], s52 offen lds\n"
      "buffer_load_dwordx4 v21, s[48:51], s52 offen offset:1024 lds\n"
      "buffer_load_dwordx4 v22, s[48:51], s52 offen offset:2048 lds\n"
      "buffer_load_dwordx4 v23, s[48:51], s52 offen offset:3072 lds\n"
      "s_add_u32 m0, m0, 4096\n"
      "s_waitcnt vmcnt(40)\n"
      "buffer_load_dwordx4 v24, s[48:51], s52 offen lds\n"
      "buffer_load_dwordx4 v25, s[48:51], s52 offen offset:1024 lds\n"
      "buffer_load_dwordx4 v26, s[48:51], s52 offen offset:2048 lds\n"
      "buffer_load_dwordx4 v27, s[48:51], s52 offen offset:3072 lds\n"
      "s_add_u32 m0, m0, 4096\n"
      "s_nop 0\n"
      "buffer_load_dwordx4 v28, s[48:51], s52 offen lds\n"
      ".Lgq_pad736:\n"
      "s_cbranch_execz .Lgq_pad2609\n"
      "s_mov_b32 m0, s64\n"
      "s_mov_b64 exec, 3\n"
      "buffer_load_dword v31, s[32:35], 0 offen lds\n"
      "s_mov_b64 exec, -1\n"
      "s_add_u32 s64, s64, 16\n"
      "s_add_u32 s18, s18, 18432\n"
      "s_add_u32 s61, s61, 2\n"
      "s_cmp_lg_u32 s61, 16\n"
      "s_cbranch_scc1 .Lgq_wo1\n"
      "s_waitcnt vmcnt(0)\n"
      "s_endpgm\n"
      ".Lgq_compute:\n"
      "s_sub_u32 s33, s22, 2\n"
      "s_lshl_b32 s26, s33, 9\n"
      "s_add_u32 s26, s26, 156160\n"
      "v_lshl_add_u32 v11, v1, 2, s26\n"
      "v_mov_b32 v12, 0x7ffffff0\n"
      "s_add_u32 m0, s26, 252\n"
      "v_lshlrev_b32 v10, 3, v1\n"
      "v_add_u32 v10, 155648, v10\n"
      "v_and_b32 v2, 15, v1\n"
      "v_lshrrev_b32 v3, 4, v1\n"
      "v_lshrrev_b32 v14, 5, v1\n"
      "v_bfe_u32 v15, v1, 2, 3\n"
      "v_lshlrev_b32 v15, 7, v15\n"
      "v_lshl_or_b32 v15, v14, 10, v15\n"
      "v_and_b32 v16, 3, v1\n"
      "v_lshl_or_b32 v15, v16, 3, v15\n"
      "v_bfe_u32 v16, v1, 3, 2\n"
      "v_lshl_or_b32 v206, v16, 5, v15\n"
      "v_xor_b32 v207, 32, v206\n"
      "v_xor_b32 v208, 64, v206\n"
      "v_xor_b32 v209, 96, v206\n"
      "v_xor_b32 v210, 65536, v206\n"
      "v_xor_b32 v211, 65568, v206\n"
      "v_xor_b32 v212, 65600, v206\n"
      "v_xor_b32 v213, 65632, v206\n"
      "v_xor_b32 v214, 131072, v206\n"
      "v_xor_b32 v215, 131104, v206\n"
      "v_xor_b32 v216, 131136, v206\n"
      "v_xor_b32 v217, 131168, v206\n"
      "v_lshrrev_b32 v15, 3, v1\n"
      "v_and_b32 v15, 2, v15\n"
      "v_or_b32 v14, v14, v15\n"
      "v_lshrrev_b32 v15, 1, v1\n"
      "v_and_b32 v15, 4, v15\n"
      "v_and_b32 v16, 2, v1\n"
      "v_bfe_u32 v17, v1, 2, 1\n"
      "v_or3_b32 v15, v15, v16, v17\n"
      "v_xor_b32 v14, v14, v15\n"
      "v_lshlrev_b32 v14, 4, v14\n"
      "v_lshl_or_b32 v218, v2, 7, v14\n"
      "v_xor_b32 v219, 64, v218\n"
      "v_xor_b32 v220, 65536, v218\n"
      "v_xor_b32 v221, 65600, v218\n"
      "v_xor_b32 v222, 131072, v218\n"
      "v_xor_b32 v223, 131136, v218\n"
      "s_lshl_b32 s27, s33, 11\n"
      "v_add_u32 v224, s27, v218\n"
      "v_add_u32 v225, s27, v219\n"
      "v_add_u32 v226, s27, v220\n"
      "v_add_u32 v227, s27, v221\n"
      "v_add_u32 v228, s27, v222\n"
      "v_add_u32 v229, s27, v223\n"
      "v_mov_b32 v132, 0\n"
      "v_mov_b32 v133, 0\n"
      "v_mov_b32 v134, 0\n"
      "v_mov_b32 v135, 0\n"
      "v_mov_b32 v136, 0\n"
      "v_mov_b32 v137, 0\n"
      "v_mov_b32 v138, 0\n"
      "v_mov_b32 v139, 0\n"
      "v_mov_b32 v140, 0\n"
      "v_mov_b32 v141, 0\n"
      "v_mov_b32 v142, 0\n"
      "v_mov_b32 v143, 0\n"
      "v_mov_b32 v144, 0\n"
      "v_mov_b32 v145, 0\n"
      "v_mov_b32 v146, 0\n"
      "v_mov_b32 v147, 0\n"
      "v_mov_b32 v148, 0\n"
      "v_mov_b32 v149, 0\n"
      "v_mov_b32 v150, 0\n"
      "v_mov_b32 v151, 0\n"
      "v_mov_b32 v152, 0\n"
      "v_mov_b32 v153, 0\n"
      "v_mov_b32 v154, 0\n"
      "v_mov_b32 v155, 0\n"
      "v_mov_b32 v156, 0\n"
      "v_mov_b32 v157, 0\n"
      "v_mov_b32 v158, 0\n"
      "v_mov_b32 v159, 0\n"
      "v_mov_b32 v160, 0\n"
      "v_mov_b32 v161, 0\n"
      "v_mov_b32 v162, 0\n"
      "v_mov_b32 v163, 0\n"
      "v_mov_b32 v164, 0\n"
      "v_mov_b32 v165, 0\n"
      "v_mov_b32 v166, 0\n"
      "v_mov_b32 v167, 0\n"
      "v_mov_b32 v28, 0x3f803f80\n"
      "v_mov_b32 v29, 0x3f803f80\n"
      "v_mov_b32 v30, 0x3f803f80\n"
      "v_mov_b32 v31, 0x3f803f80\n"
      "s_mov_b32 s35, 0x40004000\n"
      "v_lshrrev_b32 v14, 3, v2\n"
      "v_and_b32 v15, 7, v2\n"
      "v_mul_u32_u24 v14, 20480, v14\n"
      "v_lshl_or_b32 v15, v3, 3, v15\n"
      "v_lshl_add_u32 v13, v15, 4, v14\n"
      "v_lshlrev_b32 v4, 4, v1\n"
      "v_lshlrev_b32 v5, 3, v1\n"
      "v_add_u32 v5, 4096, v5\n"
      "v_lshrrev_b32 v14, 2, v1\n"
      "v_and_b32 v15, 3, v1\n"
      "v_mul_u32_u24 v14, 288, v14\n"
      "v_lshl_add_u32 v232, v15, 3, v14\n"
      "v_add_u32 v233, 64512, v232\n"
      "v_add_u32 v234, 129024, v232\n"
      "s_lshl4_add_u32 s26, s66, s33\n"
      "s_xor_b32 s27, s67, 0\n"
      "s_lshl2_add_u32 s27, s27, s26\n"
      "s_mul_i32 s44, s27, 4608\n"
      "s_add_u32 s44, s44, 4096\n"
      "s_xor_b32 s27, s67, 1\n"
      "s_lshl2_add_u32 s27, s27, s26\n"
      "s_mul_i32 s45, s27, 4608\n"
      "s_add_u32 s45, s45, 4096\n"
      "s_xor_b32 s27, s67, 2\n"
      "s_lshl2_add_u32 s27, s27, s26\n"
      "s_mul_i32 s46, s27, 4608\n"
      "s_add_u32 s46, s46, 4096\n"
      "s_xor_b32 s27, s67, 3\n"
      "s_lshl2_add_u32 s27, s27, s26\n"
      "s_mul_i32 s47, s27, 4608\n"
      "s_add_u32 s47, s47, 4096\n"
      "s_lshl3_add_u32 s26, s20, s66\n"
      "s_mul_i32 s50, s26, 9216\n"
      "s_mul_i32 s27, s67, 288\n"
      "s_add_u32 s50, s50, s27\n"
      "s_lshl3_add_u32 s50, s33, s50\n"
      "s_mul_i32 s26, s67, 163840\n"
      "s_add_u32 s26, s26, 593920\n"
      "s_lshl4_add_u32 s27, s33, s66\n"
      "s_mul_i32 s48, s27, 2560\n"
      "s_add_u32 s48, s48, s26\n"
      "s_mul_i32 s49, s66, 20480\n"
      "s_lshl_b32 s27, s33, 7\n"
      "s_add_u32 s49, s49, s27\n"
      "s_add_u32 s49, s49, s26\n"
      "s_cmp_lt_u32 s33, 4\n"
      "s_cbranch_scc1 .Lgq_walked\n"
      "s_waitcnt vmcnt(0)\n"
      "s_mov_b64 exec, 0\n"
      "s_cmp_eq_u32 s33, 5\n"
      "s_cbranch_scc1 .Lgq_pad_0\n"
      "s_branch .Lgq_pad_1\n"
      ".Lgq_walked:\n"
      "s_mov_b64 exec, -1\n"
      "s_cmp_ge_u32 s33, 4\n"
      "s_cbranch_scc1 .Lgq_extra\n"
      "s_setprio 2\n"
      "s_mul_i32 s26, s20, 2134144\n"
      "s_waitcnt lgkmcnt(0)\n"
      "s_load_dwordx2 s[24:25], s[16:17], s26\n"
      "s_add_u32 s28, s16, s26\n"
      "s_addc_u32 s29, s17, 0\n"
      "s_and_b32 s29, s29, 0xffff\n"
      "s_mov_b32 s30, 1249280\n"
      "s_mov_b32 s31, 0x20000\n"
      "s_mov_b32 s52, s14\n"
      "s_and_b32 s53, s15, 0xffff\n"
      "s_mov_b32 s54, 589824\n"
      "s_mov_b32 s55, 0x20000\n"
      ".Lgq_w10:\n"
      "ds_read_b64 v[14:15], v10\n"
      "s_sub_u32 s23, s23, 1\n"
      "s_cbranch_scc1 .Lgq_end\n"
      "s_waitcnt lgkmcnt(0)\n"
      "v_cmp_eq_u64 s[36:37], 0, v[14:15]\n"
      "s_bitcmp1_b64 s[36:37], 0\n"
      "s_cbranch_scc0 .Lgq_w10\n"
      "ds_read_b128 v[16:19], v228 offset:8192\n"
      "ds_read_b64_tr_b16 v[32:33], v214 offset:16384\n"
      "ds_read_b64_tr_b16 v[34:35], v214 offset:18432\n"
      "ds_read_b64_tr_b16 v[36:37], v215 offset:16384\n"
      "ds_read_b64_tr_b16 v[38:39], v215 offset:18432\n"
      "ds_read_b64_tr_b16 v[40:41], v216 offset:16384\n"
      "ds_read_b64_tr_b16 v[42:43], v216 offset:18432\n"
      "ds_read_b64_tr_b16 v[44:45], v217 offset:16384\n"
      "ds_read_b64_tr_b16 v[46:47], v217 offset:18432\n"
      "ds_read_b64_tr_b16 v[48:49], v214 offset:20480\n"
      "ds_read_b64_tr_b16 v[50:51], v214 offset:22528\n"
      "ds_read_b64_tr_b16 v[52:53], v215 offset:20480\n"
      "ds_read_b64_tr_b16 v[54:55], v215 offset:22528\n"
      "ds_read_b64_tr_b16 v[56:57], v216 offset:20480\n"
      "ds_read_b64_tr_b16 v[58:59], v216 offset:22528\n"
      "ds_read_b64_tr_b16 v[60:61], v217 offset:20480\n"
      "ds_read_b64_tr_b16 v[62:63], v217 offset:22528\n"
      "s_add_u32 s24, s24, 0x7f4a7c15\n"
      "s_addc_u32 s25, s25, 0x9e3779b9\n"
      "s_waitcnt lgkmcnt(8)\n"
      ".Lgq_pad_0:\n"
      ".Lgq_pad947:\n"
      "s_cbranch_execz .Lgq_pad996\n"
      "v_mfma_f32_16x16x32_bf16 v[96:99], v[32:35], v[16:19], 0\n"
      "s_bitcmp1_b64 s[36:37], 1\n"
      "s_cbranch_scc0 .Lgq_w11\n"
      ".Lgq_r11:\n"
      "ds_read_b128 v[20:23], v229 offset:8192\n"
      "ds_read_b64_tr_b16 v[64:65], v206 offset:0\n"
      "ds_read_b64_tr_b16 v[66:67], v206 offset:2048\n"
      "v_mfma_f32_16x16x32_bf16 v[100:103], v[36:39], v[16:19], 0\n"
      "ds_read_b64_tr_b16 v[68:69], v207 offset:0\n"
      "ds_read_b64_tr_b16 v[70:71], v207 offset:2048\n"
      "v_mfma_f32_16x16x32_bf16 v[104:107], v[40:43], v[16:19], 0\n"
      "ds_read_b64_tr_b16 v[72:73], v208 offset:0\n"
      "ds_read_b64_tr_b16 v[74:75], v208 offset:2048\n"
      "v_mfma_f32_16x16x32_bf16 v[108:111], v[44:47], v[16:19], 0\n"
      "s_waitcnt lgkmcnt(7)\n"
      "ds_read_b64_tr_b16 v[76:77], v209 offset:0\n"
      "ds_read_b64_tr_b16 v[78:79], v209 offset:2048\n"
      "v_mfma_f32_16x16x32_bf16 v[112:115], v[48:51], v[16:19], 0\n"
      "ds_read_b64_tr_b16 v[80:81], v206 offset:4096\n"
      "ds_read_b64_tr_b16 v[82:83], v206 offset:6144\n"
      "v_mfma_f32_16x16x32_bf16 v[116:119], v[52:55], v[16:19], 0\n"
      "ds_read_b64_tr_b16 v[84:85], v207 offset:4096\n"
      "ds_read_b64_tr_b16 v[86:87], v207 offset:6144\n"
      "v_mfma_f32_16x16x32_bf16 v[120:123], v[56:59], v[16:19], 0\n"
      "ds_read_b64_tr_b16 v[88:89], v208 offset:4096\n"
      "ds_read_b64_tr_b16 v[90:91], v208 offset:6144\n"
      "v_mfma_f32_16x16x32_bf16 v[124:127], v[60:63], v[16:19], 0\n"
      "s_waitcnt lgkmcnt(6)\n"
      "ds_read_b64_tr_b16 v[92:93], v209 offset:4096\n"
      "ds_read_b64_tr_b16 v[94:95], v209 offset:6144\n"
      "v_mfma_f32_16x16x32_bf16 v[96:99], v[64:67], v[20:23], v[96:99]\n"
      "s_bitcmp1_b64 s[36:37], 2\n"
      "s_cbranch_scc0 .Lgq_w12\n"
      ".Lgq_r12:\n"
      "ds_read_b128 v[16:19], v224 offset:8192\n"
      "ds_read_b64_tr_b16 v[32:33], v206 offset:16384\n"
      "ds_read_b64_tr_b16 v[34:35], v206 offset:18432\n"
      "v_mfma_f32_16x16x32_bf16 v[100:103], v[68:71], v[20:23], v[100:103]\n"
      "ds_read_b64_tr_b16 v[36:37], v207 offset:16384\n"
      "ds_read_b64_tr_b16 v[38:39], v207 offset:18432\n"
      "v_mfma_f32_16x16x32_bf16 v[104:107], v[72:75], v[20:23], v[104:107]\n"
      "ds_read_b64_tr_b16 v[40:41], v208 offset:16384\n"
      "ds_read_b64_tr_b16 v[42:43], v208 offset:18432\n"
      "v_mfma_f32_16x16x32_bf16 v[108:111], v[76:79], v[20:23], v[108:111]\n"
      "s_waitcnt lgkmcnt(7)\n"
      "ds_read_b64_tr_b16 v[44:45], v209 offset:16384\n"
      "ds_read_b64_tr_b16 v[46:47], v209 offset:18432\n"
      "v_mfma_f32_16x16x32_bf16 v[112:115], v[80:83], v[20:23], v[112:115]\n"
      ".Lgq_pad996:\n"
      "s_cbranch_execz .Lgq_pad1039\n"
      "buffer_load_dword v12, s[28:31], 0 offen lds\n"
      "s_sub_u32 m0, m0, 4\n"
      "ds_read_b64_tr_b16 v[48:49], v206 offset:20480\n"
      "ds_read_b64_tr_b16 v[50:51], v206 offset:22528\n"
      "v_mfma_f32_16x16x32_bf16 v[116:119], v[84:87], v[20:23], v[116:119]\n"
      "ds_read_b64_tr_b16 v[52:53], v207 offset:20480\n"
      "ds_read_b64_tr_b16 v[54:55], v207 offset:22528\n"
      "v_mfma_f32_16x16x32_bf16 v[120:123], v[88:91], v[20:23], v[120:123]\n"
      "ds_read_b64_tr_b16 v[56:57], v208 offset:20480\n"
      "ds_read_b64_tr_b16 v[58:59], v208 offset:22528\n"
      "v_mfma_f32_16x16x32_bf16 v[124:127], v[92:95], v[20:23], v[124:127]\n"
      "s_waitcnt lgkmcnt(6)\n"
      "ds_read_b64_tr_b16 v[60:61], v209 offset:20480\n"
      "ds_read_b64_tr_b16 v[62:63], v209 offset:22528\n"
      "v_mfma_f32_16x16x32_bf16 v[96:99], v[32:35], v[16:19], v[96:99]\n"
      "ds_read_b128 v[20:23], v225 offset:8192\n"
      "ds_read_b64_tr_b16 v[64:65], v206 offset:24576\n"
      "ds_read_b64_tr_b16 v[66:67], v206 offset:26624\n"
      "v_mfma_f32_16x16x32_bf16 v[100:103], v[36:39], v[16:19], v[100:103]\n"
      "ds_read_b64_tr_b16 v[68:69], v207 offset:24576\n"
      "ds_read_b64_tr_b16 v[70:71], v207 offset:26624\n"
      "v_mfma_f32_16x16x32_bf16 v[104:107], v[40:43], v[16:19], v[104:107]\n"
      "ds_read_b64_tr_b16 v[72:73], v208 offset:24576\n"
      "ds_read_b64_tr_b16 v[74:75], v208 offset:26624\n"
      "v_mfma_f32_16x16x32_bf16 v[108:111], v[44:47], v[16:19], v[108:111]\n"
      "s_waitcnt lgkmcnt(7)\n"
      "ds_read_b64_tr_b16 v[76:77], v209 offset:24576\n"
      "ds_read_b64_tr_b16 v[78:79], v209 offset:26624\n"
      "v_mfma_f32_16x16x32_bf16 v[112:115], v[48:51], v[16:19], v[112:115]\n"
      "ds_read_b64_tr_b16 v[80:81], v206 offset:28672\n"
      "ds_read_b64_tr_b16 v[82:83], v206 offset:30720\n"
      "v_mfma_f32_16x16x32_bf16 v[116:119], v[52:55], v[16:19], v[116:119]\n"
      "ds_read_b64_tr_b16 v[84:85], v207 offset:28672\n"
      "ds_read_b64_tr_b16 v[86:87], v207 offset:30720\n"
      "v_mfma_f32_16x16x32_bf16 v[120:123], v[56:59], v[16:19], v[120:123]\n"
      "ds_read_b64_tr_b16 v[88:89], v208 offset:28672\n"
      "ds_read_b64_tr_b16 v[90:91], v208 offset:30720\n"
      "v_mfma_f32_16x16x32_bf16 v[124:127], v[60:63], v[16:19], v[124:127]\n"
      "s_waitcnt lgkmcnt(6)\n"
      "ds_read_b64_tr_b16 v[92:93], v209 offset:28672\n"
      "ds_read_b64_tr_b16 v[94:95], v209 offset:30720\n"
      "v_mfma_f32_16x16x32_bf16 v[96:99], v[64:67], v[20:23], v[96:99]\n"
      ".Lgq_pad1039:\n"
      "s_cbranch_execz .Lgq_pad1086\n"
      "s_bitcmp1_b64 s[36:37], 3\n"
      "s_cbranch_scc0 .Lgq_w13\n"
      ".Lgq_r13:\n"
      "ds_read_b128 v[16:19], v224 offset:32768\n"
      "ds_read_b64_tr_b16 v[32:33], v206 offset:40960\n"
      "ds_read_b64_tr_b16 v[34:35], v206 offset:43008\n"
      "v_mfma_f32_16x16x32_bf16 v[100:103], v[68:71], v[20:23], v[100:103]\n"
      "ds_read_b64_tr_b16 v[36:37], v207 offset:40960\n"
      "ds_read_b64_tr_b16 v[38:39], v207 offset:43008\n"
      "v_mfma_f32_16x16x32_bf16 v[104:107], v[72:75], v[20:23], v[104:107]\n"
      "ds_read_b64_tr_b16 v[40:41], v208 offset:40960\n"
      "ds_read_b64_tr_b16 v[42:43], v208 offset:43008\n"
      "v_mfma_f32_16x16x32_bf16 v[108:111], v[76:79], v[20:23], v[108:111]\n"
      "s_waitcnt lgkmcnt(7)\n"
      "ds_read_b64_tr_b16 v[44:45], v209 offset:40960\n"
      "ds_read_b64_tr_b16 v[46:47], v209 offset:43008\n"
      "v_mfma_f32_16x16x32_bf16 v[112:115], v[80:83], v[20:23], v[112:115]\n"
      "buffer_load_dword v12, s[28:31], 0 offen lds\n"
      "s_sub_u32 m0, m0, 4\n"
      "ds_read_b64_tr_b16 v[48:49], v206 offset:45056\n"
      "ds_read_b64_tr_b16 v[50:51], v206 offset:47104\n"
      "v_mfma_f32_16x16x32_bf16 v[116:119], v[84:87], v[20:23], v[116:119]\n"
      "ds_read_b64_tr_b16 v[52:53], v207 offset:45056\n"
      "ds_read_b64_tr_b16 v[54:55], v207 offset:47104\n"
      "v_mfma_f32_16x16x32_bf16 v[120:123], v[88:91], v[20:23], v[120:123]\n"
      "ds_read_b64_tr_b16 v[56:57], v208 offset:45056\n"
      "ds_read_b64_tr_b16 v[58:59], v208 offset:47104\n"
      "v_mfma_f32_16x16x32_bf16 v[124:127], v[92:95], v[20:23], v[124:127]\n"
      "s_waitcnt lgkmcnt(6)\n"
      "ds_read_b64_tr_b16 v[60:61], v209 offset:45056\n"
      "ds_read_b64_tr_b16 v[62:63], v209 offset:47104\n"
      "v_mfma_f32_16x16x32_bf16 v[96:99], v[32:35], v[16:19], v[96:99]\n"
      "ds_read_b128 v[20:23], v225 offset:32768\n"
      "ds_read_b64_tr_b16 v[64:65], v206 offset:49152\n"
      "ds_read_b64_tr_b16 v[66:67], v206 offset:51200\n"
      "v_mfma_f32_16x16x32_bf16 v[100:103], v[36:39], v[16:19], v[100:103]\n"
      "ds_read_b64_tr_b16 v[68:69], v207 offset:49152\n"
      "ds_read_b64_tr_b16 v[70:71], v207 offset:51200\n"
      "v_mfma_f32_16x16x32_bf16 v[104:107], v[40:43], v[16:19], v[104:107]\n"
      "ds_read_b64_tr_b16 v[72:73], v208 offset:49152\n"
      "ds_read_b64_tr_b16 v[74:75], v208 offset:51200\n"
      "v_mfma_f32_16x16x32_bf16 v[108:111], v[44:47], v[16:19], v[108:111]\n"
      "s_waitcnt lgkmcnt(7)\n"
      "ds_read_b64_tr_b16 v[76:77], v209 offset:49152\n"
      "ds_read_b64_tr_b16 v[78:79], v209 offset:51200\n"
      "v_mfma_f32_16x16x32_bf16 v[112:115], v[48:51], v[16:19], v[112:115]\n"
      ".Lgq_pad1086:\n"
      "s_cbranch_execz .Lgq_pad1128\n"
      "ds_read_b64_tr_b16 v[80:81], v206 offset:53248\n"
      "ds_read_b64_tr_b16 v[82:83], v206 offset:55296\n"
      "v_mfma_f32_16x16x32_bf16 v[116:119], v[52:55], v[16:19], v[116:119]\n"
      "ds_read_b64_tr_b16 v[84:85], v207 offset:53248\n"
      "ds_read_b64_tr_b16 v[86:87], v207 offset:55296\n"
      "v_mfma_f32_16x16x32_bf16 v[120:123], v[56:59], v[16:19], v[120:123]\n"
      "ds_read_b64_tr_b16 v[88:89], v208 offset:53248\n"
      "ds_read_b64_tr_b16 v[90:91], v208 offset:55296\n"
      "v_mfma_f32_16x16x32_bf16 v[124:127], v[60:63], v[16:19], v[124:127]\n"
      "s_waitcnt lgkmcnt(6)\n"
      "ds_read_b64_tr_b16 v[92:93], v209 offset:53248\n"
      "ds_read_b64_tr_b16 v[94:95], v209 offset:55296\n"
      "v_mfma_f32_16x16x32_bf16 v[96:99], v[64:67], v[20:23], v[96:99]\n"
      "s_bitcmp1_b64 s[36:37], 4\n"
      "s_cbranch_scc0 .Lgq_w14\n"
      ".Lgq_r14:\n"
      "ds_read_b128 v[16:19], v224 offset:57344\n"
      "ds_read_b64_tr_b16 v[32:33], v210 offset:0\n"
      "ds_read_b64_tr_b16 v[34:35], v210 offset:2048\n"
      "v_mfma_f32_16x16x32_bf16 v[100:103], v[68:71], v[20:23], v[100:103]\n"
      "ds_read_b64_tr_b16 v[36:37], v211 offset:0\n"
      "ds_read_b64_tr_b16 v[38:39], v211 offset:2048\n"
      "v_mfma_f32_16x16x32_bf16 v[104:107], v[72:75], v[20:23], v[104:107]\n"
      "ds_read_b64_tr_b16 v[40:41], v212 offset:0\n"
      "ds_read_b64_tr_b16 v[42:43], v212 offset:2048\n"
      "v_mfma_f32_16x16x32_bf16 v[108:111], v[76:79], v[20:23], v[108:111]\n"
      "s_waitcnt lgkmcnt(7)\n"
      "ds_read_b64_tr_b16 v[44:45], v213 offset:0\n"
      "ds_read_b64_tr_b16 v[46:47], v213 offset:2048\n"
      "v_mfma_f32_16x16x32_bf16 v[112:115], v[80:83], v[20:23], v[112:115]\n"
      "buffer_load_dword v12, s[28:31], 0 offen lds\n"
      "s_sub_u32 m0, m0, 4\n"
      "ds_read_b64_tr_b16 v[48:49], v210 offset:4096\n"
      "ds_read_b64_tr_b16 v[50:51], v210 offset:6144\n"
      "v_mfma_f32_16x16x32_bf16 v[116:119], v[84:87], v[20:23], v[116:119]\n"
      "ds_read_b64_tr_b16 v[52:53], v211 offset:4096\n"
      "ds_read_b64_tr_b16 v[54:55], v211 offset:6144\n"
      "v_mfma_f32_16x16x32_bf16 v[120:123], v[88:91], v[20:23], v[120:123]\n"
      "ds_read_b64_tr_b16 v[56:57], v212 offset:4096\n"
      "ds_read_b64_tr_b16 v[58:59], v212 offset:6144\n"
      "v_mfma_f32_16x16x32_bf16 v[124:127], v[92:95], v[20:23], v[124:127]\n"
      ".Lgq_pad1128:\n"
      "s_cbranch_execz .Lgq_pad1173\n"
      "s_waitcnt lgkmcnt(6)\n"
      "ds_read_b64_tr_b16 v[60:61], v213 offset:4096\n"
      "ds_read_b64_tr_b16 v[62:63], v213 offset:6144\n"
      "v_mfma_f32_16x16x32_bf16 v[96:99], v[32:35], v[16:19], v[96:99]\n"
      "ds_read_b128 v[20:23], v225 offset:57344\n"
      "ds_read_b64_tr_b16 v[64:65], v210 offset:8192\n"
      "ds_read_b64_tr_b16 v[66:67], v210 offset:10240\n"
      "v_mfma_f32_16x16x32_bf16 v[100:103], v[36:39], v[16:19], v[100:103]\n"
      "ds_read_b64_tr_b16 v[68:69], v211 offset:8192\n"
      "ds_read_b64_tr_b16 v[70:71], v211 offset:10240\n"
      "v_mfma_f32_16x16x32_bf16 v[104:107], v[40:43], v[16:19], v[104:107]\n"
      "ds_read_b64_tr_b16 v[72:73], v212 offset:8192\n"
      "ds_read_b64_tr_b16 v[74:75], v212 offset:10240\n"
      "v_mfma_f32_16x16x32_bf16 v[108:111], v[44:47], v[16:19], v[108:111]\n"
      "s_waitcnt lgkmcnt(7)\n"
      "ds_read_b64_tr_b16 v[76:77], v213 offset:8192\n"
      "ds_read_b64_tr_b16 v[78:79], v213 offset:10240\n"
      "v_mfma_f32_16x16x32_bf16 v[112:115], v[48:51], v[16:19], v[112:115]\n"
      "ds_read_b64_tr_b16 v[80:81], v210 offset:12288\n"
      "ds_read_b64_tr_b16 v[82:83], v210 offset:14336\n"
      "v_mfma_f32_16x16x32_bf16 v[116:119], v[52:55], v[16:19], v[116:119]\n"
      "ds_read_b64_tr_b16 v[84:85], v211 offset:12288\n"
      "ds_read_b64_tr_b16 v[86:87], v211 offset:14336\n"
      "v_mfma_f32_16x16x32_bf16 v[120:123], v[56:59], v[16:19], v[120:123]\n"
      "ds_read_b64_tr_b16 v[88:89], v212 offset:12288\n"
      "ds_read_b64_tr_b16 v[90:91], v212 offset:14336\n"
      "v_mfma_f32_16x16x32_bf16 v[124:127], v[60:63], v[16:19], v[124:127]\n"
      "s_waitcnt lgkmcnt(6)\n"
      "ds_read_b64_tr_b16 v[92:93], v213 offset:12288\n"
      "ds_read_b64_tr_b16 v[94:95], v213 offset:14336\n"
      "v_mfma_f32_16x16x32_bf16 v[96:99], v[64:67], v[20:23], v[96:99]\n"
      "s_bitcmp1_b64 s[36:37], 5\n"
      "s_cbranch_scc0 .Lgq_w15\n"
      ".Lgq_r15:\n"
      "ds_read_b128 v[16:19], v226 offset:16384\n"
      "ds_read_b64_tr_b16 v[32:33], v210 offset:24576\n"
      "ds_read_b64_tr_b16 v[34:35], v210 offset:26624\n"
      "v_mfma_f32_16x16x32_bf16 v[100:103], v[68:71], v[20:23], v[100:103]\n"
      "ds_read_b64_tr_b16 v[36:37], v211 offset:24576\n"
      "ds_read_b64_tr_b16 v[38:39], v211 offset:26624\n"
      "v_mfma_f32_16x16x32_bf16 v[104:107], v[72:75], v[20:23], v[104:107]\n"
      "ds_read_b64_tr_b16 v[40:41], v212 offset:24576\n"
      "ds_read_b64_tr_b16 v[42:43], v212 offset:26624\n"
      "v_mfma_f32_16x16x32_bf16 v[108:111], v[76:79], v[20:23], v[108:111]\n"
      ".Lgq_pad1173:\n"
      "s_cbranch_execz .Lgq_pad1216\n"
      "s_waitcnt lgkmcnt(7)\n"
      "ds_read_b64_tr_b16 v[44:45], v213 offset:24576\n"
      "ds_read_b64_tr_b16 v[46:47], v213 offset:26624\n"
      "v_mfma_f32_16x16x32_bf16 v[112:115], v[80:83], v[20:23], v[112:115]\n"
      "buffer_load_dword v12, s[28:31], 0 offen lds\n"
      "s_sub_u32 m0, m0, 4\n"
      "ds_read_b64_tr_b16 v[48:49], v210 offset:28672\n"
      "ds_read_b64_tr_b16 v[50:51], v210 offset:30720\n"
      "v_mfma_f32_16x16x32_bf16 v[116:119], v[84:87], v[20:23], v[116:119]\n"
      "ds_read_b64_tr_b16 v[52:53], v211 offset:28672\n"
      "ds_read_b64_tr_b16 v[54:55], v211 offset:30720\n"
      "v_mfma_f32_16x16x32_bf16 v[120:123], v[88:91], v[20:23], v[120:123]\n"
      "ds_read_b64_tr_b16 v[56:57], v212 offset:28672\n"
      "ds_read_b64_tr_b16 v[58:59], v212 offset:30720\n"
      "v_mfma_f32_16x16x32_bf16 v[124:127], v[92:95], v[20:23], v[124:127]\n"
      "s_waitcnt lgkmcnt(6)\n"
      "ds_read_b64_tr_b16 v[60:61], v213 offset:28672\n"
      "ds_read_b64_tr_b16 v[62:63], v213 offset:30720\n"
      "v_mfma_f32_16x16x32_bf16 v[96:99], v[32:35], v[16:19], v[96:99]\n"
      "ds_read_b128 v[20:23], v227 offset:16384\n"
      "ds_read_b64_tr_b16 v[64:65], v210 offset:32768\n"
      "ds_read_b64_tr_b16 v[66:67], v210 offset:34816\n"
      "v_mfma_f32_16x16x32_bf16 v[100:103], v[36:39], v[16:19], v[100:103]\n"
      "ds_read_b64_tr_b16 v[68:69], v211 offset:32768\n"
      "ds_read_b64_tr_b16 v[70:71], v211 offset:34816\n"
      "v_mfma_f32_16x16x32_bf16 v[104:107], v[40:43], v[16:19], v[104:107]\n"
      "ds_read_b64_tr_b16 v[72:73], v212 offset:32768\n"
      "ds_read_b64_tr_b16 v[74:75], v212 offset:34816\n"
      "v_mfma_f32_16x16x32_bf16 v[108:111], v[44:47], v[16:19], v[108:111]\n"
      "s_waitcnt lgkmcnt(7)\n"
      "ds_read_b64_tr_b16 v[76:77], v213 offset:32768\n"
      "ds_read_b64_tr_b16 v[78:79], v213 offset:34816\n"
      "v_mfma_f32_16x16x32_bf16 v[112:115], v[48:51], v[16:19], v[112:115]\n"
      "ds_read_b64_tr_b16 v[80:81], v210 offset:36864\n"
      "ds_read_b64_tr_b16 v[82:83], v210 offset:38912\n"
      "v_mfma_f32_16x16x32_bf16 v[116:119], v[52:55], v[16:19], v[116:119]\n"
      "ds_read_b64_tr_b16 v[84:85], v211 offset:36864\n"
      "ds_read_b64_tr_b16 v[86:87], v211 offset:38912\n"
      "v_mfma_f32_16x16x32_bf16 v[120:123], v[56:59], v[16:19], v[120:123]\n"
      "ds_read_b64_tr_b16 v[88:89], v212 offset:36864\n"
      "ds_read_b64_tr_b16 v[90:91], v212 offset:38912\n"
      "v_mfma_f32_16x16x32_bf16 v[124:127], v[60:63], v[16:19], v[124:127]\n"
      ".Lgq_pad1216:\n"
      "s_cbranch_execz .Lgq_pad1260\n"
      "s_waitcnt lgkmcnt(6)\n"
      "ds_read_b64_tr_b16 v[92:93], v213 offset:36864\n"
      "ds_read_b64_tr_b16 v[94:95], v213 offset:38912\n"
      "v_mfma_f32_16x16x32_bf16 v[96:99], v[64:67], v[20:23], v[96:99]\n"
      "s_bitcmp1_b64 s[36:37], 6\n"
      "s_cbranch_scc0 .Lgq_w16\n"
      ".Lgq_r16:\n"
      "ds_read_b128 v[16:19], v226 offset:40960\n"
      "ds_read_b64_tr_b16 v[32:33], v210 offset:49152\n"
      "ds_read_b64_tr_b16 v[34:35], v210 offset:51200\n"
      "v_mfma_f32_16x16x32_bf16 v[100:103], v[68:71], v[20:23], v[100:103]\n"
      "ds_read_b64_tr_b16 v[36:37], v211 offset:49152\n"
      "ds_read_b64_tr_b16 v[38:39], v211 offset:51200\n"
      "v_mfma_f32_16x16x32_bf16 v[104:107], v[72:75], v[20:23], v[104:107]\n"
      "ds_read_b64_tr_b16 v[40:41], v212 offset:49152\n"
      "ds_read_b64_tr_b16 v[42:43], v212 offset:51200\n"
      "v_mfma_f32_16x16x32_bf16 v[108:111], v[76:79], v[20:23], v[108:111]\n"
      "s_waitcnt lgkmcnt(7)\n"
      "ds_read_b64_tr_b16 v[44:45], v213 offset:49152\n"
      "ds_read_b64_tr_b16 v[46:47], v213 offset:51200\n"
      "v_mfma_f32_16x16x32_bf16 v[112:115], v[80:83], v[20:23], v[112:115]\n"
      "buffer_load_dword v12, s[28:31], 0 offen lds\n"
      "s_sub_u32 m0, m0, 4\n"
      "ds_read_b64_tr_b16 v[48:49], v210 offset:53248\n"
      "ds_read_b64_tr_b16 v[50:51], v210 offset:55296\n"
      "v_mfma_f32_16x16x32_bf16 v[116:119], v[84:87], v[20:23], v[116:119]\n"
      "ds_read_b64_tr_b16 v[52:53], v211 offset:53248\n"
      "ds_read_b64_tr_b16 v[54:55], v211 offset:55296\n"
      "v_mfma_f32_16x16x32_bf16 v[120:123], v[88:91], v[20:23], v[120:123]\n"
      "ds_read_b64_tr_b16 v[56:57], v212 offset:53248\n"
      "ds_read_b64_tr_b16 v[58:59], v212 offset:55296\n"
      "v_mfma_f32_16x16x32_bf16 v[124:127], v[92:95], v[20:23], v[124:127]\n"
      "s_waitcnt lgkmcnt(6)\n"
      "ds_read_b64_tr_b16 v[60:61], v213 offset:53248\n"
      "ds_read_b64_tr_b16 v[62:63], v213 offset:55296\n"
      "v_mfma_f32_16x16x32_bf16 v[96:99], v[32:35], v[16:19], v[96:99]\n"
      "ds_read_b128 v[20:23], v227 offset:40960\n"
      "ds_read_b64_tr_b16 v[64:65], v210 offset:57344\n"
      "ds_read_b64_tr_b16 v[66:67], v210 offset:59392\n"
      "v_mfma_f32_16x16x32_bf16 v[100:103], v[36:39], v[16:19], v[100:103]\n"
      "ds_read_b64_tr_b16 v[68:69], v211 offset:57344\n"
      "ds_read_b64_tr_b16 v[70:71], v211 offset:59392\n"
      "v_mfma_f32_16x16x32_bf16 v[104:107], v[40:43], v[16:19], v[104:107]\n"
      ".Lgq_pad1260:\n"
      "s_cbranch_execz .Lgq_pad1306\n"
      "ds_read_b64_tr_b16 v[72:73], v212 offset:57344\n"
      "ds_read_b64_tr_b16 v[74:75], v212 offset:59392\n"
      "v_mfma_f32_16x16x32_bf16 v[108:111], v[44:47], v[16:19], v[108:111]\n"
      "s_waitcnt lgkmcnt(7)\n"
      "ds_read_b64_tr_b16 v[76:77], v213 offset:57344\n"
      "ds_read_b64_tr_b16 v[78:79], v213 offset:59392\n"
      "v_mfma_f32_16x16x32_bf16 v[112:115], v[48:51], v[16:19], v[112:115]\n"
      "ds_read_b64_tr_b16 v[80:81], v210 offset:61440\n"
      "ds_read_b64_tr_b16 v[82:83], v210 offset:63488\n"
      "v_mfma_f32_16x16x32_bf16 v[116:119], v[52:55], v[16:19], v[116:119]\n"
      "ds_read_b64_tr_b16 v[84:85], v211 offset:61440\n"
      "ds_read_b64_tr_b16 v[86:87], v211 offset:63488\n"
      "v_mfma_f32_16x16x32_bf16 v[120:123], v[56:59], v[16:19], v[120:123]\n"
      "ds_read_b64_tr_b16 v[88:89], v212 offset:61440\n"
      "ds_read_b64_tr_b16 v[90:91], v212 offset:63488\n"
      "v_mfma_f32_16x16x32_bf16 v[124:127], v[60:63], v[16:19], v[124:127]\n"
      "s_waitcnt lgkmcnt(6)\n"
      "ds_read_b64_tr_b16 v[92:93], v213 offset:61440\n"
      "ds_read_b64_tr_b16 v[94:95], v213 offset:63488\n"
      "v_mfma_f32_16x16x32_bf16 v[96:99], v[64:67], v[20:23], v[96:99]\n"
      "s_bitcmp1_b64 s[36:37], 7\n"
      "s_cbranch_scc0 .Lgq_w17\n"
      ".Lgq_r17:\n"
      "ds_read_b128 v[16:19], v228 offset:0\n"
      "ds_read_b64_tr_b16 v[32:33], v214 offset:8192\n"
      "ds_read_b64_tr_b16 v[34:35], v214 offset:10240\n"
      "v_mfma_f32_16x16x32_bf16 v[100:103], v[68:71], v[20:23], v[100:103]\n"
      "ds_read_b64_tr_b16 v[36:37], v215 offset:8192\n"
      "ds_read_b64_tr_b16 v[38:39], v215 offset:10240\n"
      "v_mfma_f32_16x16x32_bf16 v[104:107], v[72:75], v[20:23], v[104:107]\n"
      "ds_read_b64_tr_b16 v[40:41], v216 offset:8192\n"
      "ds_read_b64_tr_b16 v[42:43], v216 offset:10240\n"
      "v_mfma_f32_16x16x32_bf16 v[108:111], v[76:79], v[20:23], v[108:111]\n"
      "s_waitcnt lgkmcnt(7)\n"
      "ds_read_b64_tr_b16 v[44:45], v217 offset:8192\n"
      "ds_read_b64_tr_b16 v[46:47], v217 offset:10240\n"
      "v_mfma_f32_16x16x32_bf16 v[112:115], v[80:83], v[20:23], v[112:115]\n"
      "buffer_load_dword v12, s[28:31], 0 offen lds\n"
      "s_sub_u32 m0, m0, 4\n"
      "ds_read_b64_tr_b16 v[48:49], v214 offset:12288\n"
      "ds_read_b64_tr_b16 v[50:51], v214 offset:14336\n"
      "v_mfma_f32_16x16x32_bf16 v[116:119], v[84:87], v[20:23], v[116:119]\n"
      "ds_read_b64_tr_b16 v[52:53], v215 offset:12288\n"
      "ds_read_b64_tr_b16 v[54:55], v215 offset:14336\n"
      "v_mfma_f32_16x16x32_bf16 v[120:123], v[88:91], v[20:23], v[120:123]\n"
      ".Lgq_pad1306:\n"
      "s_cbranch_execz .Lgq_pad1315\n"
      "ds_read_b64_tr_b16 v[56:57], v216 offset:12288\n"
      "ds_read_b64_tr_b16 v[58:59], v216 offset:14336\n"
      "v_mfma_f32_16x16x32_bf16 v[124:127], v[92:95], v[20:23], v[124:127]\n"
      "s_waitcnt lgkmcnt(6)\n"
      "ds_read_b64_tr_b16 v[60:61], v217 offset:12288\n"
      "ds_read_b64_tr_b16 v[62:63], v217 offset:14336\n"
      "v_mfma_f32_16x16x32_bf16 v[96:99], v[32:35], v[16:19], v[96:99]\n"
      "ds_read_b128 v[20:23], v229 offset:0\n"
      ".Lgq_pad1315:\n"
      "s_cbranch_execz .Lgq_pad1359\n"
      "ds_read_b64_tr_b16 v[64:65], v214 offset:16384\n"
      "ds_read_b64_tr_b16 v[66:67], v214 offset:18432\n"
      "v_mfma_f32_16x16x32_bf16 v[100:103], v[36:39], v[16:19], v[100:103]\n"
      "ds_read_b64_tr_b16 v[68:69], v215 offset:16384\n"
      "ds_read_b64_tr_b16 v[70:71], v215 offset:18432\n"
      "v_mfma_f32_16x16x32_bf16 v[104:107], v[40:43], v[16:19], v[104:107]\n"
      "ds_read_b64_tr_b16 v[72:73], v216 offset:16384\n"
      "ds_read_b64_tr_b16 v[74:75], v216 offset:18432\n"
      "v_mfma_f32_16x16x32_bf16 v[108:111], v[44:47], v[16:19], v[108:111]\n"
      "s_waitcnt lgkmcnt(7)\n"
      "ds_read_b64_tr_b16 v[76:77], v217 offset:16384\n"
      "ds_read_b64_tr_b16 v[78:79], v217 offset:18432\n"
      "v_mfma_f32_16x16x32_bf16 v[112:115], v[48:51], v[16:19], v[112:115]\n"
      "ds_read_b64_tr_b16 v[80:81], v214 offset:20480\n"
      "ds_read_b64_tr_b16 v[82:83], v214 offset:22528\n"
      "v_mfma_f32_16x16x32_bf16 v[116:119], v[52:55], v[16:19], v[116:119]\n"
      "ds_read_b64_tr_b16 v[84:85], v215 offset:20480\n"
      "ds_read_b64_tr_b16 v[86:87], v215 offset:22528\n"
      "v_mfma_f32_16x16x32_bf16 v[120:123], v[56:59], v[16:19], v[120:123]\n"
      "ds_read_b64_tr_b16 v[88:89], v216 offset:20480\n"
      "ds_read_b64_tr_b16 v[90:91], v216 offset:22528\n"
      "v_mfma_f32_16x16x32_bf16 v[124:127], v[60:63], v[16:19], v[124:127]\n"
      "s_waitcnt lgkmcnt(6)\n"
      "ds_read_b64_tr_b16 v[92:93], v217 offset:20480\n"
      "ds_read_b64_tr_b16 v[94:95], v217 offset:22528\n"
      "v_mfma_f32_16x16x32_bf16 v[96:99], v[64:67], v[20:23], v[96:99]\n"
      "s_bitcmp1_b64 s[36:37], 8\n"
      "s_cbranch_scc0 .Lgq_w18\n"
      ".Lgq_r18:\n"
      "ds_read_b128 v[16:19], v224 offset:0\n"
      "ds_read_b64_tr_b16 v[32:33], v206 offset:8192\n"
      "ds_read_b64_tr_b16 v[34:35], v206 offset:10240\n"
      "v_mfma_f32_16x16x32_bf16 v[100:103], v[68:71], v[20:23], v[100:103]\n"
      "ds_read_b64_tr_b16 v[36:37], v207 offset:8192\n"
      "ds_read_b64_tr_b16 v[38:39], v207 offset:10240\n"
      "v_mfma_f32_16x16x32_bf16 v[104:107], v[72:75], v[20:23], v[104:107]\n"
      "ds_read_b64_tr_b16 v[40:41], v208 offset:8192\n"
      "ds_read_b64_tr_b16 v[42:43], v208 offset:10240\n"
      "v_mfma_f32_16x16x32_bf16 v[108:111], v[76:79], v[20:23], v[108:111]\n"
      "s_waitcnt lgkmcnt(7)\n"
      "ds_read_b64_tr_b16 v[44:45], v209 offset:8192\n"
      "ds_read_b64_tr_b16 v[46:47], v209 offset:10240\n"
      "v_mfma_f32_16x16x32_bf16 v[112:115], v[80:83], v[20:23], v[112:115]\n"
      ".Lgq_pad1359:\n"
      "s_cbranch_execz .Lgq_pad1398\n"
      "buffer_load_dword v12, s[28:31], 0 offen lds\n"
      "s_sub_u32 m0, m0, 4\n"
      "ds_read_b64_tr_b16 v[48:49], v206 offset:12288\n"
      "ds_read_b64_tr_b16 v[50:51], v206 offset:14336\n"
      "v_mfma_f32_16x16x32_bf16 v[116:119], v[84:87], v[20:23], v[116:119]\n"
      "ds_read_b64_tr_b16 v[52:53], v207 offset:12288\n"
      "ds_read_b64_tr_b16 v[54:55], v207 offset:14336\n"
      "v_mfma_f32_16x16x32_bf16 v[120:123], v[88:91], v[20:23], v[120:123]\n"
      "ds_read_b64_tr_b16 v[56:57], v208 offset:12288\n"
      "ds_read_b64_tr_b16 v[58:59], v208 offset:14336\n"
      "v_mfma_f32_16x16x32_bf16 v[124:127], v[92:95], v[20:23], v[124:127]\n"
      "s_waitcnt lgkmcnt(6)\n"
      "ds_read_b64_tr_b16 v[60:61], v209 offset:12288\n"
      "ds_read_b64_tr_b16 v[62:63], v209 offset:14336\n"
      "v_mfma_f32_16x16x32_bf16 v[96:99], v[32:35], v[16:19], v[96:99]\n"
      "ds_read_b128 v[20:23], v225 offset:0\n"
      "ds_read_b64_tr_b16 v[64:65], v206 offset:16384\n"
      "ds_read_b64_tr_b16 v[66:67], v206 offset:18432\n"
      "v_mfma_f32_16x16x32_bf16 v[100:103], v[36:39], v[16:19], v[100:103]\n"
      "ds_read_b64_tr_b16 v[68:69], v207 offset:16384\n"
      "ds_read_b64_tr_b16 v[70:71], v207 offset:18432\n"
      "v_mfma_f32_16x16x32_bf16 v[104:107], v[40:43], v[16:19], v[104:107]\n"
      "ds_read_b64_tr_b16 v[72:73], v208 offset:16384\n"
      "ds_read_b64_tr_b16 v[74:75], v208 offset:18432\n"
      "v_mfma_f32_16x16x32_bf16 v[108:111], v[44:47], v[16:19], v[108:111]\n"
      "s_waitcnt lgkmcnt(7)\n"
      "ds_read_b64_tr_b16 v[76:77], v209 offset:16384\n"
      "ds_read_b64_tr_b16 v[78:79], v209 offset:18432\n"
      "v_mfma_f32_16x16x32_bf16 v[112:115], v[48:51], v[16:19], v[112:115]\n"
      "ds_read_b64_tr_b16 v[80:81], v206 offset:20480\n"
      "ds_read_b64_tr_b16 v[82:83], v206 offset:22528\n"
      "v_mfma_f32_16x16x32_bf16 v[116:119], v[52:55], v[16:19], v[116:119]\n"
      "ds_read_b64_tr_b16 v[84:85], v207 offset:20480\n"
      "ds_read_b64_tr_b16 v[86:87], v207 offset:22528\n"
      "v_mfma_f32_16x16x32_bf16 v[120:123], v[56:59], v[16:19], v[120:123]\n"
      "ds_read_b64_tr_b16 v[88:89], v208 offset:20480\n"
      "ds_read_b64_tr_b16 v[90:91], v208 offset:22528\n"
      "v_mfma_f32_16x16x32_bf16 v[124:127], v[60:63], v[16:19], v[124:127]\n"
      ".Lgq_pad1398:\n"
      "s_cbranch_execz .Lgq_pad1445\n"
      "s_waitcnt lgkmcnt(6)\n"
      "ds_read_b64_tr_b16 v[92:93], v209 offset:20480\n"
      "ds_read_b64_tr_b16 v[94:95], v209 offset:22528\n"
      "v_mfma_f32_16x16x32_bf16 v[96:99], v[64:67], v[20:23], v[96:99]\n"
      "s_bitcmp1_b64 s[36:37], 9\n"
      "s_cbranch_scc0 .Lgq_w19\n"
      ".Lgq_r19:\n"
      "ds_read_b128 v[16:19], v224 offset:24576\n"
      "ds_read_b64_tr_b16 v[32:33], v206 offset:32768\n"
      "ds_read_b64_tr_b16 v[34:35], v206 offset:34816\n"
      "v_mfma_f32_16x16x32_bf16 v[100:103], v[68:71], v[20:23], v[100:103]\n"
      "ds_read_b64_tr_b16 v[36:37], v207 offset:32768\n"
      "ds_read_b64_tr_b16 v[38:39], v207 offset:34816\n"
      "v_mfma_f32_16x16x32_bf16 v[104:107], v[72:75], v[20:23], v[104:107]\n"
      "ds_read_b64_tr_b16 v[40:41], v208 offset:32768\n"
      "ds_read_b64_tr_b16 v[42:43], v208 offset:34816\n"
      "v_mfma_f32_16x16x32_bf16 v[108:111], v[76:79], v[20:23], v[108:111]\n"
      "s_waitcnt lgkmcnt(7)\n"
      "ds_read_b64_tr_b16 v[44:45], v209 offset:32768\n"
      "ds_read_b64_tr_b16 v[46:47], v209 offset:34816\n"
      "v_mfma_f32_16x16x32_bf16 v[112:115], v[80:83], v[20:23], v[112:115]\n"
      "buffer_load_dword v12, s[28:31], 0 offen lds\n"
      "s_sub_u32 m0, m0, 4\n"
      "ds_read_b64_tr_b16 v[48:49], v206 offset:36864\n"
      "ds_read_b64_tr_b16 v[50:51], v206 offset:38912\n"
      "v_mfma_f32_16x16x32_bf16 v[116:119], v[84:87], v[20:23], v[116:119]\n"
      "ds_read_b64_tr_b16 v[52:53], v207 offset:36864\n"
      "ds_read_b64_tr_b16 v[54:55], v207 offset:38912\n"
      "v_mfma_f32_16x16x32_bf16 v[120:123], v[88:91], v[20:23], v[120:123]\n"
      "ds_read_b64_tr_b16 v[56:57], v208 offset:36864\n"
      "ds_read_b64_tr_b16 v[58:59], v208 offset:38912\n"
      "v_mfma_f32_16x16x32_bf16 v[124:127], v[92:95], v[20:23], v[124:127]\n"
      "s_waitcnt lgkmcnt(6)\n"
      "ds_read_b64_tr_b16 v[60:61], v209 offset:36864\n"
      "ds_read_b64_tr_b16 v[62:63], v209 offset:38912\n"
      "v_mfma_f32_16x16x32_bf16 v[96:99], v[32:35], v[16:19], v[96:99]\n"
      "ds_read_b128 v[20:23], v225 offset:24576\n"
      "ds_read_b64_tr_b16 v[64:65], v206 offset:40960\n"
      "ds_read_b64_tr_b16 v[66:67], v206 offset:43008\n"
      "v_mfma_f32_16x16x32_bf16 v[100:103], v[36:39], v[16:19], v[100:103]\n"
      "ds_read_b64_tr_b16 v[68:69], v207 offset:40960\n"
      "ds_read_b64_tr_b16 v[70:71], v207 offset:43008\n"
      "v_mfma_f32_16x16x32_bf16 v[104:107], v[40:43], v[16:19], v[104:107]\n"
      "ds_read_b64_tr_b16 v[72:73], v208 offset:40960\n"
      "ds_read_b64_tr_b16 v[74:75], v208 offset:43008\n"
      "v_mfma_f32_16x16x32_bf16 v[108:111], v[44:47], v[16:19], v[108:111]\n"
      ".Lgq_pad1445:\n"
      "s_cbranch_execz .Lgq_pad1474\n"
      "s_waitcnt lgkmcnt(7)\n"
      "ds_read_b64_tr_b16 v[76:77], v209 offset:40960\n"
      "ds_read_b64_tr_b16 v[78:79], v209 offset:43008\n"
      "v_mfma_f32_16x16x32_bf16 v[112:115], v[48:51], v[16:19], v[112:115]\n"
      "ds_read_b64_tr_b16 v[80:81], v206 offset:45056\n"
      "ds_read_b64_tr_b16 v[82:83], v206 offset:47104\n"
      "v_mfma_f32_16x16x32_bf16 v[116:119], v[52:55], v[16:19], v[116:119]\n"
      "ds_read_b64_tr_b16 v[84:85], v207 offset:45056\n"
      "ds_read_b64_tr_b16 v[86:87], v207 offset:47104\n"
      "v_mfma_f32_16x16x32_bf16 v[120:123], v[56:59], v[16:19], v[120:123]\n"
      "ds_read_b64_tr_b16 v[88:89], v208 offset:45056\n"
      "ds_read_b64_tr_b16 v[90:91], v208 offset:47104\n"
      "v_mfma_f32_16x16x32_bf16 v[124:127], v[60:63], v[16:19], v[124:127]\n"
      "s_waitcnt lgkmcnt(6)\n"
      "ds_read_b64_tr_b16 v[92:93], v209 offset:45056\n"
      "ds_read_b64_tr_b16 v[94:95], v209 offset:47104\n"
      "v_mfma_f32_16x16x32_bf16 v[96:99], v[64:67], v[20:23], v[96:99]\n"
      "s_mov_b32 s26, 0x3e0293ee\n"
      "v_mfma_f32_16x16x32_bf16 v[100:103], v[68:71], v[20:23], v[100:103]\n"
      "v_mfma_f32_16x16x32_bf16 v[104:107], v[72:75], v[20:23], v[104:107]\n"
      "v_mfma_f32_16x16x32_bf16 v[108:111], v[76:79], v[20:23], v[108:111]\n"
      "s_waitcnt lgkmcnt(0)\n"
      "buffer_load_dword v12, s[28:31], 0 offen lds\n"
      "s_sub_u32 m0, m0, 4\n"
      "v_mfma_f32_16x16x32_bf16 v[112:115], v[80:83], v[20:23], v[112:115]\n"
      "v_mfma_f32_16x16x32_bf16 v[116:119], v[84:87], v[20:23], v[116:119]\n"
      "v_mfma_f32_16x16x32_bf16 v[120:123], v[88:91], v[20:23], v[120:123]\n"
      "v_mfma_f32_16x16x32_bf16 v[124:127], v[92:95], v[20:23], v[124:127]\n"
      ".Lgq_pad1474:\n"
      "s_cbranch_execz .Lgq_pad1528\n"
      "v_pk_mul_f32 v[96:97], v[96:97], s[26:27] op_sel_hi:[1,0]\n"
      "v_pk_mul_f32 v[98:99], v[98:99], s[26:27] op_sel_hi:[1,0]\n"
      "v_pk_mul_f32 v[100:101], v[100:101], s[26:27] op_sel_hi:[1,0]\n"
      "v_pk_mul_f32 v[102:103], v[102:103], s[26:27] op_sel_hi:[1,0]\n"
      "v_cvt_pk_bf16_f32 v168, v96, v97\n"
      "v_cvt_pk_bf16_f32 v169, v98, v99\n"
      "v_cvt_pk_bf16_f32 v170, v100, v101\n"
      "v_cvt_pk_bf16_f32 v171, v102, v103\n"
      "s_nop 0\n"
      "v_permlane16_swap_b32 v168, v170\n"
      "v_permlane16_swap_b32 v169, v171\n"
      "v_pk_mul_f32 v[104:105], v[104:105], s[26:27] op_sel_hi:[1,0]\n"
      "v_pk_mul_f32 v[106:107], v[106:107], s[26:27] op_sel_hi:[1,0]\n"
      "v_pk_mul_f32 v[108:109], v[108:109], s[26:27] op_sel_hi:[1,0]\n"
      "v_pk_mul_f32 v[110:111], v[110:111], s[26:27] op_sel_hi:[1,0]\n"
      "v_cvt_pk_bf16_f32 v172, v104, v105\n"
      "v_cvt_pk_bf16_f32 v173, v106, v107\n"
      "v_cvt_pk_bf16_f32 v174, v108, v109\n"
      "v_cvt_pk_bf16_f32 v175, v110, v111\n"
      "s_nop 0\n"
      "v_permlane16_swap_b32 v172, v174\n"
      "v_permlane16_swap_b32 v173, v175\n"
      "v_pk_mul_f32 v[112:113], v[112:113], s[26:27] op_sel_hi:[1,0]\n"
      "v_pk_mul_f32 v[114:115], v[114:115], s[26:27] op_sel_hi:[1,0]\n"
      "v_pk_mul_f32 v[116:117], v[116:117], s[26:27] op_sel_hi:[1,0]\n"
      "v_pk_mul_f32 v[118:119], v[118:119], s[26:27] op_sel_hi:[1,0]\n"
      "v_cvt_pk_bf16_f32 v176, v112, v113\n"
      "v_cvt_pk_bf16_f32 v177, v114, v115\n"
      "v_cvt_pk_bf16_f32 v178, v116, v117\n"
      "v_cvt_pk_bf16_f32 v179, v118, v119\n"
      "s_nop 0\n"
      "v_permlane16_swap_b32 v176, v178\n"
      "v_permlane16_swap_b32 v177, v179\n"
      "s_bitcmp1_b64 s[36:37], 10\n"
      "s_cbranch_scc0 .Lgq_w20\n"
      ".Lgq_r20:\n"
      "ds_read_b128 v[32:35], v218 offset:49152\n"
      "ds_read_b128 v[48:51], v218 offset:51200\n"
      "ds_read_b128 v[36:39], v219 offset:49152\n"
      "ds_read_b128 v[52:55], v219 offset:51200\n"
      "ds_read_b128 v[40:43], v218 offset:53248\n"
      "ds_read_b128 v[56:59], v218 offset:55296\n"
      "ds_read_b128 v[44:47], v219 offset:53248\n"
      "ds_read_b128 v[60:63], v219 offset:55296\n"
      "v_pk_mul_f32 v[120:121], v[120:121], s[26:27] op_sel_hi:[1,0]\n"
      "v_pk_mul_f32 v[122:123], v[122:123], s[26:27] op_sel_hi:[1,0]\n"
      "v_pk_mul_f32 v[124:125], v[124:125], s[26:27] op_sel_hi:[1,0]\n"
      "v_pk_mul_f32 v[126:127], v[126:127], s[26:27] op_sel_hi:[1,0]\n"
      "v_cvt_pk_bf16_f32 v180, v120, v121\n"
      "v_cvt_pk_bf16_f32 v181, v122, v123\n"
      "v_cvt_pk_bf16_f32 v182, v124, v125\n"
      "v_cvt_pk_bf16_f32 v183, v126, v127\n"
      ".Lgq_pad1528:\n"
      "s_cbranch_execz .Lgq_pad1588\n"
      "s_nop 0\n"
      "v_permlane16_swap_b32 v180, v182\n"
      "v_permlane16_swap_b32 v181, v183\n"
      "s_waitcnt lgkmcnt(4)\n"
      "v_mfma_f32_16x16x32_bf16 v[184:187], v[32:35], v[168:171], 0\n"
      "v_mfma_f32_16x16x32_bf16 v[188:191], v[48:51], v[168:171], 0\n"
      "v_mfma_f32_16x16x32_bf16 v[184:187], v[36:39], v[172:175], v[184:187]\n"
      "v_mfma_f32_16x16x32_bf16 v[188:191], v[52:55], v[172:175], v[188:191]\n"
      "s_waitcnt lgkmcnt(0)\n"
      "v_mfma_f32_16x16x32_bf16 v[184:187], v[40:43], v[176:179], v[184:187]\n"
      "v_mfma_f32_16x16x32_bf16 v[188:191], v[56:59], v[176:179], v[188:191]\n"
      "v_mfma_f32_16x16x32_bf16 v[184:187], v[44:47], v[180:183], v[184:187]\n"
      "v_mfma_f32_16x16x32_bf16 v[188:191], v[60:63], v[180:183], v[188:191]\n"
      "s_bitcmp1_b64 s[36:37], 11\n"
      "s_cbranch_scc0 .Lgq_w21\n"
      ".Lgq_r21:\n"
      "ds_read_b128 v[32:35], v218 offset:57344\n"
      "ds_read_b128 v[48:51], v218 offset:59392\n"
      "ds_read_b128 v[36:39], v219 offset:57344\n"
      "ds_read_b128 v[52:55], v219 offset:59392\n"
      "ds_read_b128 v[40:43], v218 offset:61440\n"
      "ds_read_b128 v[56:59], v218 offset:63488\n"
      "ds_read_b128 v[44:47], v219 offset:61440\n"
      "ds_read_b128 v[60:63], v219 offset:63488\n"
      "v_max3_f32 v204, v184, v185, v186\n"
      "v_max3_f32 v204, v204, v187, v188\n"
      "v_max3_f32 v204, v204, v189, v190\n"
      "v_max_f32 v204, v204, v191\n"
      "v_mov_b32 v205, v204\n"
      "s_nop 1\n"
      "v_permlane16_swap_b32 v205, v204\n"
      "v_max_f32 v204, v204, v205\n"
      "v_mov_b32 v205, v204\n"
      "s_nop 1\n"
      "v_permlane32_swap_b32 v205, v204\n"
      "v_max_f32 v204, v204, v205\n"
      "v_sub_f32 v24, 0xc1700000, v204\n"
      "v_mov_b32 v25, v24\n"
      "v_mov_b32 v26, v24\n"
      "v_mov_b32 v27, v24\n"
      "v_add_f32 v184, v184, v24\n"
      "v_add_f32 v185, v185, v24\n"
      "v_add_f32 v186, v186, v24\n"
      "v_add_f32 v187, v187, v24\n"
      "v_add_f32 v188, v188, v24\n"
      "v_add_f32 v189, v189, v24\n"
      "v_add_f32 v190, v190, v24\n"
      "v_add_f32 v191, v191, v24\n"
      "v_exp_f32 v192, v184\n"
      "v_exp_f32 v193, v185\n"
      "v_exp_f32 v194, v186\n"
      "v_exp_f32 v195, v187\n"
      "v_exp_f32 v196, v188\n"
      "v_exp_f32 v197, v189\n"
      "v_exp_f32 v198, v190\n"
      "v_exp_f32 v199, v191\n"
      "v_cvt_pk_bf16_f32 v200, v192, v193\n"
      "v_cvt_pk_bf16_f32 v201, v194, v195\n"
      ".Lgq_pad1588:\n"
      "s_cbranch_execz .Lgq_pad1635\n"
      "v_cvt_pk_bf16_f32 v202, v196, v197\n"
      "v_cvt_pk_bf16_f32 v203, v198, v199\n"
      "s_waitcnt lgkmcnt(0)\n"
      "v_mfma_f32_16x16x32_bf16 v[96:99], v[32:35], v[168:171], v[24:27]\n"
      "s_bitcmp1_b64 s[36:37], 12\n"
      "s_cbranch_scc0 .Lgq_w22\n"
      ".Lgq_r22:\n"
      "ds_read_b64_tr_b16 v[64:65], v210 offset:0\n"
      "ds_read_b64_tr_b16 v[66:67], v210 offset:2048\n"
      "v_mfma_f32_16x16x32_bf16 v[100:103], v[48:51], v[168:171], v[24:27]\n"
      "ds_read_b64_tr_b16 v[68:69], v211 offset:0\n"
      "ds_read_b64_tr_b16 v[70:71], v211 offset:2048\n"
      "v_mfma_f32_16x16x32_bf16 v[96:99], v[36:39], v[172:175], v[96:99]\n"
      "ds_read_b64_tr_b16 v[72:73], v212 offset:0\n"
      "ds_read_b64_tr_b16 v[74:75], v212 offset:2048\n"
      "v_mfma_f32_16x16x32_bf16 v[100:103], v[52:55], v[172:175], v[100:103]\n"
      "s_waitcnt lgkmcnt(6)\n"
      "ds_read_b64_tr_b16 v[76:77], v213 offset:0\n"
      "ds_read_b64_tr_b16 v[78:79], v213 offset:2048\n"
      "v_mfma_f32_16x16x32_bf16 v[96:99], v[40:43], v[176:179], v[96:99]\n"
      "ds_read_b64_tr_b16 v[80:81], v210 offset:4096\n"
      "ds_read_b64_tr_b16 v[82:83], v210 offset:6144\n"
      "v_mfma_f32_16x16x32_bf16 v[100:103], v[56:59], v[176:179], v[100:103]\n"
      "ds_read_b64_tr_b16 v[84:85], v211 offset:4096\n"
      "ds_read_b64_tr_b16 v[86:87], v211 offset:6144\n"
      "v_mfma_f32_16x16x32_bf16 v[96:99], v[44:47], v[180:183], v[96:99]\n"
      "ds_read_b64_tr_b16 v[88:89], v212 offset:4096\n"
      "ds_read_b64_tr_b16 v[90:91], v212 offset:6144\n"
      "v_mfma_f32_16x16x32_bf16 v[100:103], v[60:63], v[180:183], v[100:103]\n"
      "ds_read_b64_tr_b16 v[92:93], v213 offset:4096\n"
      "ds_read_b64_tr_b16 v[94:95], v213 offset:6144\n"
      "v_mfma_f32_16x16x32_bf16 v[164:167], v[28:31], v[200:203], v[164:167]\n"
      "s_waitcnt lgkmcnt(8)\n"
      "ds_read_b128 v[32:35], v220 offset:8192\n"
      "v_mfma_f32_16x16x32_bf16 v[132:135], v[64:67], v[200:203], v[132:135]\n"
      "ds_read_b128 v[48:51], v220 offset:10240\n"
      "v_exp_f32 v192, v96\n"
      "v_mfma_f32_16x16x32_bf16 v[136:139], v[68:71], v[200:203], v[136:139]\n"
      "ds_read_b128 v[36:39], v221 offset:8192\n"
      "v_exp_f32 v193, v97\n"
      "v_mfma_f32_16x16x32_bf16 v[140:143], v[72:75], v[200:203], v[140:143]\n"
      "ds_read_b128 v[52:55], v221 offset:10240\n"
      "v_exp_f32 v194, v98\n"
      "v_mfma_f32_16x16x32_bf16 v[144:147], v[76:79], v[200:203], v[144:147]\n"
      ".Lgq_pad1635:\n"
      "s_cbranch_execz .Lgq_pad1683\n"
      "s_waitcnt lgkmcnt(4)\n"
      "ds_read_b128 v[40:43], v220 offset:12288\n"
      "v_exp_f32 v195, v99\n"
      "v_mfma_f32_16x16x32_bf16 v[148:151], v[80:83], v[200:203], v[148:151]\n"
      "ds_read_b128 v[56:59], v220 offset:14336\n"
      "v_exp_f32 v196, v100\n"
      "v_mfma_f32_16x16x32_bf16 v[152:155], v[84:87], v[200:203], v[152:155]\n"
      "ds_read_b128 v[44:47], v221 offset:12288\n"
      "v_exp_f32 v197, v101\n"
      "v_mfma_f32_16x16x32_bf16 v[156:159], v[88:91], v[200:203], v[156:159]\n"
      "ds_read_b128 v[60:63], v221 offset:14336\n"
      "v_exp_f32 v198, v102\n"
      "v_mfma_f32_16x16x32_bf16 v[160:163], v[92:95], v[200:203], v[160:163]\n"
      "s_waitcnt lgkmcnt(4)\n"
      "buffer_load_dword v12, s[28:31], 0 offen lds\n"
      "s_sub_u32 m0, m0, 4\n"
      "v_exp_f32 v199, v103\n"
      "v_mfma_f32_16x16x32_bf16 v[184:187], v[32:35], v[168:171], v[24:27]\n"
      "s_bitcmp1_b64 s[36:37], 13\n"
      "s_cbranch_scc0 .Lgq_w23\n"
      ".Lgq_r23:\n"
      "ds_read_b64_tr_b16 v[64:65], v210 offset:16384\n"
      "ds_read_b64_tr_b16 v[66:67], v210 offset:18432\n"
      "v_cvt_pk_bf16_f32 v200, v192, v193\n"
      "v_mfma_f32_16x16x32_bf16 v[188:191], v[48:51], v[168:171], v[24:27]\n"
      "ds_read_b64_tr_b16 v[68:69], v211 offset:16384\n"
      "ds_read_b64_tr_b16 v[70:71], v211 offset:18432\n"
      "v_cvt_pk_bf16_f32 v201, v194, v195\n"
      "v_mfma_f32_16x16x32_bf16 v[184:187], v[36:39], v[172:175], v[184:187]\n"
      "ds_read_b64_tr_b16 v[72:73], v212 offset:16384\n"
      "ds_read_b64_tr_b16 v[74:75], v212 offset:18432\n"
      "v_cvt_pk_bf16_f32 v202, v196, v197\n"
      "v_mfma_f32_16x16x32_bf16 v[188:191], v[52:55], v[172:175], v[188:191]\n"
      "s_waitcnt lgkmcnt(6)\n"
      "ds_read_b64_tr_b16 v[76:77], v213 offset:16384\n"
      "ds_read_b64_tr_b16 v[78:79], v213 offset:18432\n"
      "v_cvt_pk_bf16_f32 v203, v198, v199\n"
      "v_mfma_f32_16x16x32_bf16 v[184:187], v[40:43], v[176:179], v[184:187]\n"
      "ds_read_b64_tr_b16 v[80:81], v210 offset:20480\n"
      "ds_read_b64_tr_b16 v[82:83], v210 offset:22528\n"
      "v_or3_b32 v204, v200, v201, v202\n"
      "v_mfma_f32_16x16x32_bf16 v[188:191], v[56:59], v[176:179], v[188:191]\n"
      "ds_read_b64_tr_b16 v[84:85], v211 offset:20480\n"
      "ds_read_b64_tr_b16 v[86:87], v211 offset:22528\n"
      "v_bitop3_b32 v204, v204, v203, s35 bitop3:0xa8\n"
      "v_mfma_f32_16x16x32_bf16 v[184:187], v[44:47], v[180:183], v[184:187]\n"
      ".Lgq_pad1683:\n"
      "s_cbranch_execz .Lgq_pad1734\n"
      "ds_read_b64_tr_b16 v[88:89], v212 offset:20480\n"
      "ds_read_b64_tr_b16 v[90:91], v212 offset:22528\n"
      "v_cmp_ne_u32 vcc, 0, v204\n"
      "v_mfma_f32_16x16x32_bf16 v[188:191], v[60:63], v[180:183], v[188:191]\n"
      "s_cbranch_vccnz .Lgq_rare1\n"
      ".Lgq_rback1:\n"
      "ds_read_b64_tr_b16 v[92:93], v213 offset:20480\n"
      "ds_read_b64_tr_b16 v[94:95], v213 offset:22528\n"
      "v_mfma_f32_16x16x32_bf16 v[164:167], v[28:31], v[200:203], v[164:167]\n"
      "s_waitcnt lgkmcnt(8)\n"
      "ds_read_b128 v[32:35], v220 offset:24576\n"
      "v_mfma_f32_16x16x32_bf16 v[132:135], v[64:67], v[200:203], v[132:135]\n"
      "ds_read_b128 v[48:51], v220 offset:26624\n"
      "v_exp_f32 v192, v184\n"
      "v_mfma_f32_16x16x32_bf16 v[136:139], v[68:71], v[200:203], v[136:139]\n"
      "ds_read_b128 v[36:39], v221 offset:24576\n"
      "v_exp_f32 v193, v185\n"
      "v_mfma_f32_16x16x32_bf16 v[140:143], v[72:75], v[200:203], v[140:143]\n"
      "ds_read_b128 v[52:55], v221 offset:26624\n"
      "v_exp_f32 v194, v186\n"
      "v_mfma_f32_16x16x32_bf16 v[144:147], v[76:79], v[200:203], v[144:147]\n"
      "s_waitcnt lgkmcnt(4)\n"
      "ds_read_b128 v[40:43], v220 offset:28672\n"
      "v_exp_f32 v195, v187\n"
      "v_mfma_f32_16x16x32_bf16 v[148:151], v[80:83], v[200:203], v[148:151]\n"
      "ds_read_b128 v[56:59], v220 offset:30720\n"
      "v_exp_f32 v196, v188\n"
      "v_mfma_f32_16x16x32_bf16 v[152:155], v[84:87], v[200:203], v[152:155]\n"
      "ds_read_b128 v[44:47], v221 offset:28672\n"
      "v_exp_f32 v197, v189\n"
      "v_mfma_f32_16x16x32_bf16 v[156:159], v[88:91], v[200:203], v[156:159]\n"
      "ds_read_b128 v[60:63], v221 offset:30720\n"
      "v_exp_f32 v198, v190\n"
      "v_mfma_f32_16x16x32_bf16 v[160:163], v[92:95], v[200:203], v[160:163]\n"
      "s_waitcnt lgkmcnt(4)\n"
      "v_exp_f32 v199, v191\n"
      "v_mfma_f32_16x16x32_bf16 v[96:99], v[32:35], v[168:171], v[24:27]\n"
      "s_bitcmp1_b64 s[36:37], 14\n"
      "s_cbranch_scc0 .Lgq_w24\n"
      ".Lgq_r24:\n"
      "ds_read_b64_tr_b16 v[64:65], v210 offset:32768\n"
      "ds_read_b64_tr_b16 v[66:67], v210 offset:34816\n"
      "v_cvt_pk_bf16_f32 v200, v192, v193\n"
      "v_mfma_f32_16x16x32_bf16 v[100:103], v[48:51], v[168:171], v[24:27]\n"
      "ds_read_b64_tr_b16 v[68:69], v211 offset:32768\n"
      "ds_read_b64_tr_b16 v[70:71], v211 offset:34816\n"
      "v_cvt_pk_bf16_f32 v201, v194, v195\n"
      "v_mfma_f32_16x16x32_bf16 v[96:99], v[36:39], v[172:175], v[96:99]\n"
      ".Lgq_pad1734:\n"
      "s_cbranch_execz .Lgq_pad1781\n"
      "ds_read_b64_tr_b16 v[72:73], v212 offset:32768\n"
      "ds_read_b64_tr_b16 v[74:75], v212 offset:34816\n"
      "v_cvt_pk_bf16_f32 v202, v196, v197\n"
      "v_mfma_f32_16x16x32_bf16 v[100:103], v[52:55], v[172:175], v[100:103]\n"
      "s_waitcnt lgkmcnt(6)\n"
      "ds_read_b64_tr_b16 v[76:77], v213 offset:32768\n"
      "ds_read_b64_tr_b16 v[78:79], v213 offset:34816\n"
      "v_cvt_pk_bf16_f32 v203, v198, v199\n"
      "v_mfma_f32_16x16x32_bf16 v[96:99], v[40:43], v[176:179], v[96:99]\n"
      "ds_read_b64_tr_b16 v[80:81], v210 offset:36864\n"
      "ds_read_b64_tr_b16 v[82:83], v210 offset:38912\n"
      "v_or3_b32 v204, v200, v201, v202\n"
      "v_mfma_f32_16x16x32_bf16 v[100:103], v[56:59], v[176:179], v[100:103]\n"
      "ds_read_b64_tr_b16 v[84:85], v211 offset:36864\n"
      "ds_read_b64_tr_b16 v[86:87], v211 offset:38912\n"
      "v_bitop3_b32 v204, v204, v203, s35 bitop3:0xa8\n"
      "v_mfma_f32_16x16x32_bf16 v[96:99], v[44:47], v[180:183], v[96:99]\n"
      "ds_read_b64_tr_b16 v[88:89], v212 offset:36864\n"
      "ds_read_b64_tr_b16 v[90:91], v212 offset:38912\n"
      "v_cmp_ne_u32 vcc, 0, v204\n"
      "v_mfma_f32_16x16x32_bf16 v[100:103], v[60:63], v[180:183], v[100:103]\n"
      "s_cbranch_vccnz .Lgq_rare2\n"
      ".Lgq_rback2:\n"
      "ds_read_b64_tr_b16 v[92:93], v213 offset:36864\n"
      "ds_read_b64_tr_b16 v[94:95], v213 offset:38912\n"
      "v_mfma_f32_16x16x32_bf16 v[164:167], v[28:31], v[200:203], v[164:167]\n"
      "s_waitcnt lgkmcnt(8)\n"
      "ds_read_b128 v[32:35], v220 offset:40960\n"
      "v_mfma_f32_16x16x32_bf16 v[132:135], v[64:67], v[200:203], v[132:135]\n"
      "ds_read_b128 v[48:51], v220 offset:43008\n"
      "v_exp_f32 v192, v96\n"
      "v_mfma_f32_16x16x32_bf16 v[136:139], v[68:71], v[200:203], v[136:139]\n"
      "ds_read_b128 v[36:39], v221 offset:40960\n"
      "v_exp_f32 v193, v97\n"
      "v_mfma_f32_16x16x32_bf16 v[140:143], v[72:75], v[200:203], v[140:143]\n"
      "ds_read_b128 v[52:55], v221 offset:43008\n"
      "v_exp_f32 v194, v98\n"
      "v_mfma_f32_16x16x32_bf16 v[144:147], v[76:79], v[200:203], v[144:147]\n"
      "s_waitcnt lgkmcnt(4)\n"
      "ds_read_b128 v[40:43], v220 offset:45056\n"
      "v_exp_f32 v195, v99\n"
      "v_mfma_f32_16x16x32_bf16 v[148:151], v[80:83], v[200:203], v[148:151]\n"
      "ds_read_b128 v[56:59], v220 offset:47104\n"
      "v_exp_f32 v196, v100\n"
      "v_mfma_f32_16x16x32_bf16 v[152:155], v[84:87], v[200:203], v[152:155]\n"
      ".Lgq_pad1781:\n"
      "s_cbranch_execz .Lgq_pad1832\n"
      "ds_read_b128 v[44:47], v221 offset:45056\n"
      "v_exp_f32 v197, v101\n"
      "v_mfma_f32_16x16x32_bf16 v[156:159], v[88:91], v[200:203], v[156:159]\n"
      "ds_read_b128 v[60:63], v221 offset:47104\n"
      "v_exp_f32 v198, v102\n"
      "v_mfma_f32_16x16x32_bf16 v[160:163], v[92:95], v[200:203], v[160:163]\n"
      "s_waitcnt lgkmcnt(4)\n"
      "buffer_load_dword v12, s[28:31], 0 offen lds\n"
      "s_sub_u32 m0, m0, 4\n"
      "v_exp_f32 v199, v103\n"
      "v_mfma_f32_16x16x32_bf16 v[184:187], v[32:35], v[168:171], v[24:27]\n"
      "s_bitcmp1_b64 s[36:37], 15\n"
      "s_cbranch_scc0 .Lgq_w25\n"
      ".Lgq_r25:\n"
      "ds_read_b64_tr_b16 v[64:65], v210 offset:49152\n"
      "ds_read_b64_tr_b16 v[66:67], v210 offset:51200\n"
      "v_cvt_pk_bf16_f32 v200, v192, v193\n"
      "v_mfma_f32_16x16x32_bf16 v[188:191], v[48:51], v[168:171], v[24:27]\n"
      "ds_read_b64_tr_b16 v[68:69], v211 offset:49152\n"
      "ds_read_b64_tr_b16 v[70:71], v211 offset:51200\n"
      "v_cvt_pk_bf16_f32 v201, v194, v195\n"
      "v_mfma_f32_16x16x32_bf16 v[184:187], v[36:39], v[172:175], v[184:187]\n"
      "ds_read_b64_tr_b16 v[72:73], v212 offset:49152\n"
      "ds_read_b64_tr_b16 v[74:75], v212 offset:51200\n"
      "v_cvt_pk_bf16_f32 v202, v196, v197\n"
      "v_mfma_f32_16x16x32_bf16 v[188:191], v[52:55], v[172:175], v[188:191]\n"
      "s_waitcnt lgkmcnt(6)\n"
      "ds_read_b64_tr_b16 v[76:77], v213 offset:49152\n"
      "ds_read_b64_tr_b16 v[78:79], v213 offset:51200\n"
      "v_cvt_pk_bf16_f32 v203, v198, v199\n"
      "v_mfma_f32_16x16x32_bf16 v[184:187], v[40:43], v[176:179], v[184:187]\n"
      "ds_read_b64_tr_b16 v[80:81], v210 offset:53248\n"
      "ds_read_b64_tr_b16 v[82:83], v210 offset:55296\n"
      "v_or3_b32 v204, v200, v201, v202\n"
      "v_mfma_f32_16x16x32_bf16 v[188:191], v[56:59], v[176:179], v[188:191]\n"
      "ds_read_b64_tr_b16 v[84:85], v211 offset:53248\n"
      "ds_read_b64_tr_b16 v[86:87], v211 offset:55296\n"
      "v_bitop3_b32 v204, v204, v203, s35 bitop3:0xa8\n"
      "v_mfma_f32_16x16x32_bf16 v[184:187], v[44:47], v[180:183], v[184:187]\n"
      "ds_read_b64_tr_b16 v[88:89], v212 offset:53248\n"
      "ds_read_b64_tr_b16 v[90:91], v212 offset:55296\n"
      "v_cmp_ne_u32 vcc, 0, v204\n"
      "v_mfma_f32_16x16x32_bf16 v[188:191], v[60:63], v[180:183], v[188:191]\n"
      "s_cbranch_vccnz .Lgq_rare3\n"
      ".Lgq_rback3:\n"
      "ds_read_b64_tr_b16 v[92:93], v213 offset:53248\n"
      "ds_read_b64_tr_b16 v[94:95], v213 offset:55296\n"
      "v_mfma_f32_16x16x32_bf16 v[164:167], v[28:31], v[200:203], v[164:167]\n"
      ".Lgq_pad1832:\n"
      "s_cbranch_execz .Lgq_pad1882\n"
      "s_waitcnt lgkmcnt(8)\n"
      "ds_read_b128 v[32:35], v220 offset:57344\n"
      "v_mfma_f32_16x16x32_bf16 v[132:135], v[64:67], v[200:203], v[132:135]\n"
      "ds_read_b128 v[48:51], v220 offset:59392\n"
      "v_exp_f32 v192, v184\n"
      "v_mfma_f32_16x16x32_bf16 v[136:139], v[68:71], v[200:203], v[136:139]\n"
      "ds_read_b128 v[36:39], v221 offset:57344\n"
      "v_exp_f32 v193, v185\n"
      "v_mfma_f32_16x16x32_bf16 v[140:143], v[72:75], v[200:203], v[140:143]\n"
      "ds_read_b128 v[52:55], v221 offset:59392\n"
      "v_exp_f32 v194, v186\n"
      "v_mfma_f32_16x16x32_bf16 v[144:147], v[76:79], v[200:203], v[144:147]\n"
      "s_waitcnt lgkmcnt(4)\n"
      "ds_read_b128 v[40:43], v220 offset:61440\n"
      "v_exp_f32 v195, v187\n"
      "v_mfma_f32_16x16x32_bf16 v[148:151], v[80:83], v[200:203], v[148:151]\n"
      "ds_read_b128 v[56:59], v220 offset:63488\n"
      "v_exp_f32 v196, v188\n"
      "v_mfma_f32_16x16x32_bf16 v[152:155], v[84:87], v[200:203], v[152:155]\n"
      "ds_read_b128 v[44:47], v221 offset:61440\n"
      "v_exp_f32 v197, v189\n"
      "v_mfma_f32_16x16x32_bf16 v[156:159], v[88:91], v[200:203], v[156:159]\n"
      "ds_read_b128 v[60:63], v221 offset:63488\n"
      "v_exp_f32 v198, v190\n"
      "v_mfma_f32_16x16x32_bf16 v[160:163], v[92:95], v[200:203], v[160:163]\n"
      "s_waitcnt lgkmcnt(4)\n"
      "v_exp_f32 v199, v191\n"
      "v_mfma_f32_16x16x32_bf16 v[96:99], v[32:35], v[168:171], v[24:27]\n"
      "s_bitcmp1_b64 s[36:37], 16\n"
      "s_cbranch_scc0 .Lgq_w26\n"
      ".Lgq_r26:\n"
      "ds_read_b64_tr_b16 v[64:65], v214 offset:0\n"
      "ds_read_b64_tr_b16 v[66:67], v214 offset:2048\n"
      "v_cvt_pk_bf16_f32 v200, v192, v193\n"
      "v_mfma_f32_16x16x32_bf16 v[100:103], v[48:51], v[168:171], v[24:27]\n"
      "ds_read_b64_tr_b16 v[68:69], v215 offset:0\n"
      "ds_read_b64_tr_b16 v[70:71], v215 offset:2048\n"
      "v_cvt_pk_bf16_f32 v201, v194, v195\n"
      "v_mfma_f32_16x16x32_bf16 v[96:99], v[36:39], v[172:175], v[96:99]\n"
      "ds_read_b64_tr_b16 v[72:73], v216 offset:0\n"
      "ds_read_b64_tr_b16 v[74:75], v216 offset:2048\n"
      "v_cvt_pk_bf16_f32 v202, v196, v197\n"
      "v_mfma_f32_16x16x32_bf16 v[100:103], v[52:55], v[172:175], v[100:103]\n"
      "s_waitcnt lgkmcnt(6)\n"
      "ds_read_b64_tr_b16 v[76:77], v217 offset:0\n"
      "ds_read_b64_tr_b16 v[78:79], v217 offset:2048\n"
      "v_cvt_pk_bf16_f32 v203, v198, v199\n"
      "v_mfma_f32_16x16x32_bf16 v[96:99], v[40:43], v[176:179], v[96:99]\n"
      ".Lgq_pad1882:\n"
      "s_cbranch_execz .Lgq_pad1932\n"
      "ds_read_b64_tr_b16 v[80:81], v214 offset:4096\n"
      "ds_read_b64_tr_b16 v[82:83], v214 offset:6144\n"
      "v_or3_b32 v204, v200, v201, v202\n"
      "v_mfma_f32_16x16x32_bf16 v[100:103], v[56:59], v[176:179], v[100:103]\n"
      "ds_read_b64_tr_b16 v[84:85], v215 offset:4096\n"
      "ds_read_b64_tr_b16 v[86:87], v215 offset:6144\n"
      "v_bitop3_b32 v204, v204, v203, s35 bitop3:0xa8\n"
      "v_mfma_f32_16x16x32_bf16 v[96:99], v[44:47], v[180:183], v[96:99]\n"
      "ds_read_b64_tr_b16 v[88:89], v216 offset:4096\n"
      "ds_read_b64_tr_b16 v[90:91], v216 offset:6144\n"
      "v_cmp_ne_u32 vcc, 0, v204\n"
      "v_mfma_f32_16x16x32_bf16 v[100:103], v[60:63], v[180:183], v[100:103]\n"
      "s_cbranch_vccnz .Lgq_rare4\n"
      ".Lgq_rback4:\n"
      "ds_read_b64_tr_b16 v[92:93], v217 offset:4096\n"
      "ds_read_b64_tr_b16 v[94:95], v217 offset:6144\n"
      "v_mfma_f32_16x16x32_bf16 v[164:167], v[28:31], v[200:203], v[164:167]\n"
      "s_waitcnt lgkmcnt(8)\n"
      "ds_read_b128 v[32:35], v222 offset:8192\n"
      "v_mfma_f32_16x16x32_bf16 v[132:135], v[64:67], v[200:203], v[132:135]\n"
      "ds_read_b128 v[48:51], v222 offset:10240\n"
      "v_exp_f32 v192, v96\n"
      "v_mfma_f32_16x16x32_bf16 v[136:139], v[68:71], v[200:203], v[136:139]\n"
      "ds_read_b128 v[36:39], v223 offset:8192\n"
      "v_exp_f32 v193, v97\n"
      "v_mfma_f32_16x16x32_bf16 v[140:143], v[72:75], v[200:203], v[140:143]\n"
      "ds_read_b128 v[52:55], v223 offset:10240\n"
      "v_exp_f32 v194, v98\n"
      "v_mfma_f32_16x16x32_bf16 v[144:147], v[76:79], v[200:203], v[144:147]\n"
      "s_waitcnt lgkmcnt(4)\n"
      "ds_read_b128 v[40:43], v222 offset:12288\n"
      "v_exp_f32 v195, v99\n"
      "v_mfma_f32_16x16x32_bf16 v[148:151], v[80:83], v[200:203], v[148:151]\n"
      "ds_read_b128 v[56:59], v222 offset:14336\n"
      "v_exp_f32 v196, v100\n"
      "v_mfma_f32_16x16x32_bf16 v[152:155], v[84:87], v[200:203], v[152:155]\n"
      "ds_read_b128 v[44:47], v223 offset:12288\n"
      "v_exp_f32 v197, v101\n"
      "v_mfma_f32_16x16x32_bf16 v[156:159], v[88:91], v[200:203], v[156:159]\n"
      "ds_read_b128 v[60:63], v223 offset:14336\n"
      "v_exp_f32 v198, v102\n"
      "v_mfma_f32_16x16x32_bf16 v[160:163], v[92:95], v[200:203], v[160:163]\n"
      "s_waitcnt lgkmcnt(4)\n"
      "buffer_load_dword v12, s[28:31], 0 offen lds\n"
      "s_sub_u32 m0, m0, 4\n"
      "v_exp_f32 v199, v103\n"
      "v_mfma_f32_16x16x32_bf16 v[184:187], v[32:35], v[168:171], v[24:27]\n"
      ".Lgq_pad1932:\n"
      "s_cbranch_execz .Lgq_pad1980\n"
      "s_bitcmp1_b64 s[36:37], 17\n"
      "s_cbranch_scc0 .Lgq_w27\n"
      ".Lgq_r27:\n"
      "ds_read_b64_tr_b16 v[64:65], v214 offset:16384\n"
      "ds_read_b64_tr_b16 v[66:67], v214 offset:18432\n"
      "v_cvt_pk_bf16_f32 v200, v192, v193\n"
      "v_mfma_f32_16x16x32_bf16 v[188:191], v[48:51], v[168:171], v[24:27]\n"
      "ds_read_b64_tr_b16 v[68:69], v215 offset:16384\n"
      "ds_read_b64_tr_b16 v[70:71], v215 offset:18432\n"
      "v_cvt_pk_bf16_f32 v201, v194, v195\n"
      "v_mfma_f32_16x16x32_bf16 v[184:187], v[36:39], v[172:175], v[184:187]\n"
      "ds_read_b64_tr_b16 v[72:73], v216 offset:16384\n"
      "ds_read_b64_tr_b16 v[74:75], v216 offset:18432\n"
      "v_cvt_pk_bf16_f32 v202, v196, v197\n"
      "v_mfma_f32_16x16x32_bf16 v[188:191], v[52:55], v[172:175], v[188:191]\n"
      "s_waitcnt lgkmcnt(6)\n"
      "ds_read_b64_tr_b16 v[76:77], v217 offset:16384\n"
      "ds_read_b64_tr_b16 v[78:79], v217 offset:18432\n"
      "v_cvt_pk_bf16_f32 v203, v198, v199\n"
      "v_mfma_f32_16x16x32_bf16 v[184:187], v[40:43], v[176:179], v[184:187]\n"
      "ds_read_b64_tr_b16 v[80:81], v214 offset:20480\n"
      "ds_read_b64_tr_b16 v[82:83], v214 offset:22528\n"
      "v_or3_b32 v204, v200, v201, v202\n"
      "v_mfma_f32_16x16x32_bf16 v[188:191], v[56:59], v[176:179], v[188:191]\n"
      "ds_read_b64_tr_b16 v[84:85], v215 offset:20480\n"
      "ds_read_b64_tr_b16 v[86:87], v215 offset:22528\n"
      "v_bitop3_b32 v204, v204, v203, s35 bitop3:0xa8\n"
      "v_mfma_f32_16x16x32_bf16 v[184:187], v[44:47], v[180:183], v[184:187]\n"
      "ds_read_b64_tr_b16 v[88:89], v216 offset:20480\n"
      "ds_read_b64_tr_b16 v[90:91], v216 offset:22528\n"
      "v_cmp_ne_u32 vcc, 0, v204\n"
      "v_mfma_f32_16x16x32_bf16 v[188:191], v[60:63], v[180:183], v[188:191]\n"
      "s_cbranch_vccnz .Lgq_rare5\n"
      ".Lgq_rback5:\n"
      "ds_read_b64_tr_b16 v[92:93], v217 offset:20480\n"
      "ds_read_b64_tr_b16 v[94:95], v217 offset:22528\n"
      "v_mfma_f32_16x16x32_bf16 v[164:167], v[28:31], v[200:203], v[164:167]\n"
      "s_waitcnt lgkmcnt(8)\n"
      "ds_read_b128 v[32:35], v218 offset:0\n"
      "v_mfma_f32_16x16x32_bf16 v[132:135], v[64:67], v[200:203], v[132:135]\n"
      "ds_read_b128 v[48:51], v218 offset:2048\n"
      "v_exp_f32 v192, v184\n"
      "v_mfma_f32_16x16x32_bf16 v[136:139], v[68:71], v[200:203], v[136:139]\n"
      "ds_read_b128 v[36:39], v219 offset:0\n"
      "v_exp_f32 v193, v185\n"
      "v_mfma_f32_16x16x32_bf16 v[140:143], v[72:75], v[200:203], v[140:143]\n"
      ".Lgq_pad1980:\n"
      "s_cbranch_execz .Lgq_pad2025\n"
      "ds_read_b128 v[52:55], v219 offset:2048\n"
      "v_exp_f32 v194, v186\n"
      "v_mfma_f32_16x16x32_bf16 v[144:147], v[76:79], v[200:203], v[144:147]\n"
      "s_waitcnt lgkmcnt(4)\n"
      "ds_read_b128 v[40:43], v218 offset:4096\n"
      "v_exp_f32 v195, v187\n"
      "v_mfma_f32_16x16x32_bf16 v[148:151], v[80:83], v[200:203], v[148:151]\n"
      "ds_read_b128 v[56:59], v218 offset:6144\n"
      "v_exp_f32 v196, v188\n"
      "v_mfma_f32_16x16x32_bf16 v[152:155], v[84:87], v[200:203], v[152:155]\n"
      "ds_read_b128 v[44:47], v219 offset:4096\n"
      "v_exp_f32 v197, v189\n"
      "v_mfma_f32_16x16x32_bf16 v[156:159], v[88:91], v[200:203], v[156:159]\n"
      "ds_read_b128 v[60:63], v219 offset:6144\n"
      "v_exp_f32 v198, v190\n"
      "v_mfma_f32_16x16x32_bf16 v[160:163], v[92:95], v[200:203], v[160:163]\n"
      "s_waitcnt lgkmcnt(4)\n"
      "v_exp_f32 v199, v191\n"
      "v_mfma_f32_16x16x32_bf16 v[96:99], v[32:35], v[168:171], v[24:27]\n"
      "s_bitcmp1_b64 s[36:37], 18\n"
      "s_cbranch_scc0 .Lgq_w28\n"
      ".Lgq_r28:\n"
      "ds_read_b64_tr_b16 v[64:65], v206 offset:8192\n"
      "ds_read_b64_tr_b16 v[66:67], v206 offset:10240\n"
      "v_cvt_pk_bf16_f32 v200, v192, v193\n"
      "v_mfma_f32_16x16x32_bf16 v[100:103], v[48:51], v[168:171], v[24:27]\n"
      "ds_read_b64_tr_b16 v[68:69], v207 offset:8192\n"
      "ds_read_b64_tr_b16 v[70:71], v207 offset:10240\n"
      "v_cvt_pk_bf16_f32 v201, v194, v195\n"
      "v_mfma_f32_16x16x32_bf16 v[96:99], v[36:39], v[172:175], v[96:99]\n"
      "ds_read_b64_tr_b16 v[72:73], v208 offset:8192\n"
      "ds_read_b64_tr_b16 v[74:75], v208 offset:10240\n"
      "v_cvt_pk_bf16_f32 v202, v196, v197\n"
      "v_mfma_f32_16x16x32_bf16 v[100:103], v[52:55], v[172:175], v[100:103]\n"
      "s_waitcnt lgkmcnt(6)\n"
      "ds_read_b64_tr_b16 v[76:77], v209 offset:8192\n"
      "ds_read_b64_tr_b16 v[78:79], v209 offset:10240\n"
      "v_cvt_pk_bf16_f32 v203, v198, v199\n"
      "v_mfma_f32_16x16x32_bf16 v[96:99], v[40:43], v[176:179], v[96:99]\n"
      "ds_read_b64_tr_b16 v[80:81], v206 offset:12288\n"
      "ds_read_b64_tr_b16 v[82:83], v206 offset:14336\n"
      "v_or3_b32 v204, v200, v201, v202\n"
      "v_mfma_f32_16x16x32_bf16 v[100:103], v[56:59], v[176:179], v[100:103]\n"
      ".Lgq_pad2025:\n"
      "s_cbranch_execz .Lgq_pad2078\n"
      "ds_read_b64_tr_b16 v[84:85], v207 offset:12288\n"
      "ds_read_b64_tr_b16 v[86:87], v207 offset:14336\n"
      "v_bitop3_b32 v204, v204, v203, s35 bitop3:0xa8\n"
      "v_mfma_f32_16x16x32_bf16 v[96:99], v[44:47], v[180:183], v[96:99]\n"
      "ds_read_b64_tr_b16 v[88:89], v208 offset:12288\n"
      "ds_read_b64_tr_b16 v[90:91], v208 offset:14336\n"
      "v_cmp_ne_u32 vcc, 0, v204\n"
      "v_mfma_f32_16x16x32_bf16 v[100:103], v[60:63], v[180:183], v[100:103]\n"
      "s_cbranch_vccnz .Lgq_rare6\n"
      ".Lgq_rback6:\n"
      "ds_read_b64_tr_b16 v[92:93], v209 offset:12288\n"
      "ds_read_b64_tr_b16 v[94:95], v209 offset:14336\n"
      "v_mfma_f32_16x16x32_bf16 v[164:167], v[28:31], v[200:203], v[164:167]\n"
      "s_waitcnt lgkmcnt(8)\n"
      "ds_read_b128 v[32:35], v218 offset:16384\n"
      "v_mfma_f32_16x16x32_bf16 v[132:135], v[64:67], v[200:203], v[132:135]\n"
      "ds_read_b128 v[48:51], v218 offset:18432\n"
      "v_exp_f32 v192, v96\n"
      "v_mfma_f32_16x16x32_bf16 v[136:139], v[68:71], v[200:203], v[136:139]\n"
      "ds_read_b128 v[36:39], v219 offset:16384\n"
      "v_exp_f32 v193, v97\n"
      "v_mfma_f32_16x16x32_bf16 v[140:143], v[72:75], v[200:203], v[140:143]\n"
      "ds_read_b128 v[52:55], v219 offset:18432\n"
      "v_exp_f32 v194, v98\n"
      "v_mfma_f32_16x16x32_bf16 v[144:147], v[76:79], v[200:203], v[144:147]\n"
      "s_waitcnt lgkmcnt(4)\n"
      "ds_read_b128 v[40:43], v218 offset:20480\n"
      "v_exp_f32 v195, v99\n"
      "v_mfma_f32_16x16x32_bf16 v[148:151], v[80:83], v[200:203], v[148:151]\n"
      "ds_read_b128 v[56:59], v218 offset:22528\n"
      "v_exp_f32 v196, v100\n"
      "v_mfma_f32_16x16x32_bf16 v[152:155], v[84:87], v[200:203], v[152:155]\n"
      "ds_read_b128 v[44:47], v219 offset:20480\n"
      "v_exp_f32 v197, v101\n"
      "v_mfma_f32_16x16x32_bf16 v[156:159], v[88:91], v[200:203], v[156:159]\n"
      "ds_read_b128 v[60:63], v219 offset:22528\n"
      "v_exp_f32 v198, v102\n"
      "v_mfma_f32_16x16x32_bf16 v[160:163], v[92:95], v[200:203], v[160:163]\n"
      "s_waitcnt lgkmcnt(4)\n"
      "buffer_load_dword v12, s[28:31], 0 offen lds\n"
      "s_sub_u32 m0, m0, 4\n"
      "v_exp_f32 v199, v103\n"
      "v_mfma_f32_16x16x32_bf16 v[184:187], v[32:35], v[168:171], v[24:27]\n"
      "s_bitcmp1_b64 s[36:37], 19\n"
      "s_cbranch_scc0 .Lgq_w29\n"
      ".Lgq_r29:\n"
      "ds_read_b64_tr_b16 v[64:65], v206 offset:24576\n"
      "ds_read_b64_tr_b16 v[66:67], v206 offset:26624\n"
      "v_cvt_pk_bf16_f32 v200, v192, v193\n"
      "v_mfma_f32_16x16x32_bf16 v[188:191], v[48:51], v[168:171], v[24:27]\n"
      ".Lgq_pad2078:\n"
      "s_cbranch_execz .Lgq_pad2126\n"
      "ds_read_b64_tr_b16 v[68:69], v207 offset:24576\n"
      "ds_read_b64_tr_b16 v[70:71], v207 offset:26624\n"
      "v_cvt_pk_bf16_f32 v201, v194, v195\n"
      "v_mfma_f32_16x16x32_bf16 v[184:187], v[36:39], v[172:175], v[184:187]\n"
      "ds_read_b64_tr_b16 v[72:73], v208 offset:24576\n"
      "ds_read_b64_tr_b16 v[74:75], v208 offset:26624\n"
      "v_cvt_pk_bf16_f32 v202, v196, v197\n"
      "v_mfma_f32_16x16x32_bf16 v[188:191], v[52:55], v[172:175], v[188:191]\n"
      "s_waitcnt lgkmcnt(6)\n"
      "ds_read_b64_tr_b16 v[76:77], v209 offset:24576\n"
      "ds_read_b64_tr_b16 v[78:79], v209 offset:26624\n"
      "v_cvt_pk_bf16_f32 v203, v198, v199\n"
      "v_mfma_f32_16x16x32_bf16 v[184:187], v[40:43], v[176:179], v[184:187]\n"
      "ds_read_b64_tr_b16 v[80:81], v206 offset:28672\n"
      "ds_read_b64_tr_b16 v[82:83], v206 offset:30720\n"
      "v_or3_b32 v204, v200, v201, v202\n"
      "v_mfma_f32_16x16x32_bf16 v[188:191], v[56:59], v[176:179], v[188:191]\n"
      "ds_read_b64_tr_b16 v[84:85], v207 offset:28672\n"
      "ds_read_b64_tr_b16 v[86:87], v207 offset:30720\n"
      "v_bitop3_b32 v204, v204, v203, s35 bitop3:0xa8\n"
      "v_mfma_f32_16x16x32_bf16 v[184:187], v[44:47], v[180:183], v[184:187]\n"
      "ds_read_b64_tr_b16 v[88:89], v208 offset:28672\n"
      "ds_read_b64_tr_b16 v[90:91], v208 offset:30720\n"
      "v_cmp_ne_u32 vcc, 0, v204\n"
      "v_mfma_f32_16x16x32_bf16 v[188:191], v[60:63], v[180:183], v[188:191]\n"
      "s_cbranch_vccnz .Lgq_rare7\n"
      ".Lgq_rback7:\n"
      "ds_read_b64_tr_b16 v[92:93], v209 offset:28672\n"
      "ds_read_b64_tr_b16 v[94:95], v209 offset:30720\n"
      "v_mfma_f32_16x16x32_bf16 v[164:167], v[28:31], v[200:203], v[164:167]\n"
      "s_waitcnt lgkmcnt(8)\n"
      "ds_read_b128 v[32:35], v218 offset:32768\n"
      "v_mfma_f32_16x16x32_bf16 v[132:135], v[64:67], v[200:203], v[132:135]\n"
      "ds_read_b128 v[48:51], v218 offset:34816\n"
      "v_exp_f32 v192, v184\n"
      "v_mfma_f32_16x16x32_bf16 v[136:139], v[68:71], v[200:203], v[136:139]\n"
      "ds_read_b128 v[36:39], v219 offset:32768\n"
      "v_exp_f32 v193, v185\n"
      "v_mfma_f32_16x16x32_bf16 v[140:143], v[72:75], v[200:203], v[140:143]\n"
      "ds_read_b128 v[52:55], v219 offset:34816\n"
      "v_exp_f32 v194, v186\n"
      "v_mfma_f32_16x16x32_bf16 v[144:147], v[76:79], v[200:203], v[144:147]\n"
      "s_waitcnt lgkmcnt(4)\n"
      "ds_read_b128 v[40:43], v218 offset:36864\n"
      "v_exp_f32 v195, v187\n"
      "v_mfma_f32_16x16x32_bf16 v[148:151], v[80:83], v[200:203], v[148:151]\n"
      ".Lgq_pad2126:\n"
      "s_cbranch_execz .Lgq_pad2172\n"
      "ds_read_b128 v[56:59], v218 offset:38912\n"
      "v_exp_f32 v196, v188\n"
      "v_mfma_f32_16x16x32_bf16 v[152:155], v[84:87], v[200:203], v[152:155]\n"
      "ds_read_b128 v[44:47], v219 offset:36864\n"
      "v_exp_f32 v197, v189\n"
      "v_mfma_f32_16x16x32_bf16 v[156:159], v[88:91], v[200:203], v[156:159]\n"
      "ds_read_b128 v[60:63], v219 offset:38912\n"
      "v_exp_f32 v198, v190\n"
      "v_mfma_f32_16x16x32_bf16 v[160:163], v[92:95], v[200:203], v[160:163]\n"
      "s_waitcnt lgkmcnt(4)\n"
      "v_exp_f32 v199, v191\n"
      "v_mfma_f32_16x16x32_bf16 v[96:99], v[32:35], v[168:171], v[24:27]\n"
      "s_bitcmp1_b64 s[36:37], 20\n"
      "s_cbranch_scc0 .Lgq_w30\n"
      ".Lgq_r30:\n"
      "ds_read_b64_tr_b16 v[64:65], v206 offset:40960\n"
      "ds_read_b64_tr_b16 v[66:67], v206 offset:43008\n"
      "v_cvt_pk_bf16_f32 v200, v192, v193\n"
      "v_mfma_f32_16x16x32_bf16 v[100:103], v[48:51], v[168:171], v[24:27]\n"
      "ds_read_b64_tr_b16 v[68:69], v207 offset:40960\n"
      "ds_read_b64_tr_b16 v[70:71], v207 offset:43008\n"
      "v_cvt_pk_bf16_f32 v201, v194, v195\n"
      "v_mfma_f32_16x16x32_bf16 v[96:99], v[36:39], v[172:175], v[96:99]\n"
      "ds_read_b64_tr_b16 v[72:73], v208 offset:40960\n"
      "ds_read_b64_tr_b16 v[74:75], v208 offset:43008\n"
      "v_cvt_pk_bf16_f32 v202, v196, v197\n"
      "v_mfma_f32_16x16x32_bf16 v[100:103], v[52:55], v[172:175], v[100:103]\n"
      "s_waitcnt lgkmcnt(6)\n"
      "ds_read_b64_tr_b16 v[76:77], v209 offset:40960\n"
      "ds_read_b64_tr_b16 v[78:79], v209 offset:43008\n"
      "v_cvt_pk_bf16_f32 v203, v198, v199\n"
      "v_mfma_f32_16x16x32_bf16 v[96:99], v[40:43], v[176:179], v[96:99]\n"
      "ds_read_b64_tr_b16 v[80:81], v206 offset:45056\n"
      "ds_read_b64_tr_b16 v[82:83], v206 offset:47104\n"
      "v_or3_b32 v204, v200, v201, v202\n"
      "v_mfma_f32_16x16x32_bf16 v[100:103], v[56:59], v[176:179], v[100:103]\n"
      "ds_read_b64_tr_b16 v[84:85], v207 offset:45056\n"
      "ds_read_b64_tr_b16 v[86:87], v207 offset:47104\n"
      "v_bitop3_b32 v204, v204, v203, s35 bitop3:0xa8\n"
      "v_mfma_f32_16x16x32_bf16 v[96:99], v[44:47], v[180:183], v[96:99]\n"
      "ds_read_b64_tr_b16 v[88:89], v208 offset:45056\n"
      "ds_read_b64_tr_b16 v[90:91], v208 offset:47104\n"
      "v_cmp_ne_u32 vcc, 0, v204\n"
      "v_mfma_f32_16x16x32_bf16 v[100:103], v[60:63], v[180:183], v[100:103]\n"
      ".Lgq_pad2172:\n"
      "s_cbranch_execz .Lgq_pad2225\n"
      "s_cbranch_vccnz .Lgq_rare8\n"
      ".Lgq_rback8:\n"
      "ds_read_b64_tr_b16 v[92:93], v209 offset:45056\n"
      "ds_read_b64_tr_b16 v[94:95], v209 offset:47104\n"
      "v_mfma_f32_16x16x32_bf16 v[164:167], v[28:31], v[200:203], v[164:167]\n"
      "s_waitcnt lgkmcnt(8)\n"
      "ds_read_b128 v[32:35], v218 offset:49152\n"
      "v_mfma_f32_16x16x32_bf16 v[132:135], v[64:67], v[200:203], v[132:135]\n"
      "ds_read_b128 v[48:51], v218 offset:51200\n"
      "v_exp_f32 v192, v96\n"
      "v_mfma_f32_16x16x32_bf16 v[136:139], v[68:71], v[200:203], v[136:139]\n"
      "ds_read_b128 v[36:39], v219 offset:49152\n"
      "v_exp_f32 v193, v97\n"
      "v_mfma_f32_16x16x32_bf16 v[140:143], v[72:75], v[200:203], v[140:143]\n"
      "ds_read_b128 v[52:55], v219 offset:51200\n"
      "v_exp_f32 v194, v98\n"
      "v_mfma_f32_16x16x32_bf16 v[144:147], v[76:79], v[200:203], v[144:147]\n"
      "s_waitcnt lgkmcnt(4)\n"
      "ds_read_b128 v[40:43], v218 offset:53248\n"
      "v_exp_f32 v195, v99\n"
      "v_mfma_f32_16x16x32_bf16 v[148:151], v[80:83], v[200:203], v[148:151]\n"
      "ds_read_b128 v[56:59], v218 offset:55296\n"
      "v_exp_f32 v196, v100\n"
      "v_mfma_f32_16x16x32_bf16 v[152:155], v[84:87], v[200:203], v[152:155]\n"
      "ds_read_b128 v[44:47], v219 offset:53248\n"
      "v_exp_f32 v197, v101\n"
      "v_mfma_f32_16x16x32_bf16 v[156:159], v[88:91], v[200:203], v[156:159]\n"
      "ds_read_b128 v[60:63], v219 offset:55296\n"
      "v_exp_f32 v198, v102\n"
      "v_mfma_f32_16x16x32_bf16 v[160:163], v[92:95], v[200:203], v[160:163]\n"
      "s_waitcnt lgkmcnt(4)\n"
      "buffer_load_dword v12, s[28:31], 0 offen lds\n"
      "s_sub_u32 m0, m0, 4\n"
      "v_exp_f32 v199, v103\n"
      "v_mfma_f32_16x16x32_bf16 v[184:187], v[32:35], v[168:171], v[24:27]\n"
      "s_bitcmp1_b64 s[36:37], 21\n"
      "s_cbranch_scc0 .Lgq_w31\n"
      ".Lgq_r31:\n"
      "ds_read_b64_tr_b16 v[64:65], v206 offset:57344\n"
      "ds_read_b64_tr_b16 v[66:67], v206 offset:59392\n"
      "v_cvt_pk_bf16_f32 v200, v192, v193\n"
      "v_mfma_f32_16x16x32_bf16 v[188:191], v[48:51], v[168:171], v[24:27]\n"
      "ds_read_b64_tr_b16 v[68:69], v207 offset:57344\n"
      "ds_read_b64_tr_b16 v[70:71], v207 offset:59392\n"
      "v_cvt_pk_bf16_f32 v201, v194, v195\n"
      "v_mfma_f32_16x16x32_bf16 v[184:187], v[36:39], v[172:175], v[184:187]\n"
      "ds_read_b64_tr_b16 v[72:73], v208 offset:57344\n"
      "ds_read_b64_tr_b16 v[74:75], v208 offset:59392\n"
      "v_cvt_pk_bf16_f32 v202, v196, v197\n"
      "v_mfma_f32_16x16x32_bf16 v[188:191], v[52:55], v[172:175], v[188:191]\n"
      ".Lgq_pad2225:\n"
      "s_cbranch_execz .Lgq_pad2274\n"
      "s_waitcnt lgkmcnt(6)\n"
      "ds_read_b64_tr_b16 v[76:77], v209 offset:57344\n"
      "ds_read_b64_tr_b16 v[78:79], v209 offset:59392\n"
      "v_cvt_pk_bf16_f32 v203, v198, v199\n"
      "v_mfma_f32_16x16x32_bf16 v[184:187], v[40:43], v[176:179], v[184:187]\n"
      "ds_read_b64_tr_b16 v[80:81], v206 offset:61440\n"
      "ds_read_b64_tr_b16 v[82:83], v206 offset:63488\n"
      "v_or3_b32 v204, v200, v201, v202\n"
      "v_mfma_f32_16x16x32_bf16 v[188:191], v[56:59], v[176:179], v[188:191]\n"
      "ds_read_b64_tr_b16 v[84:85], v207 offset:61440\n"
      "ds_read_b64_tr_b16 v[86:87], v207 offset:63488\n"
      "v_bitop3_b32 v204, v204, v203, s35 bitop3:0xa8\n"
      "v_mfma_f32_16x16x32_bf16 v[184:187], v[44:47], v[180:183], v[184:187]\n"
      "ds_read_b64_tr_b16 v[88:89], v208 offset:61440\n"
      "ds_read_b64_tr_b16 v[90:91], v208 offset:63488\n"
      "v_cmp_ne_u32 vcc, 0, v204\n"
      "v_mfma_f32_16x16x32_bf16 v[188:191], v[60:63], v[180:183], v[188:191]\n"
      "s_cbranch_vccnz .Lgq_rare9\n"
      ".Lgq_rback9:\n"
      "ds_read_b64_tr_b16 v[92:93], v209 offset:61440\n"
      "ds_read_b64_tr_b16 v[94:95], v209 offset:63488\n"
      "v_mfma_f32_16x16x32_bf16 v[164:167], v[28:31], v[200:203], v[164:167]\n"
      "s_waitcnt lgkmcnt(8)\n"
      "ds_read_b128 v[32:35], v220 offset:0\n"
      "v_mfma_f32_16x16x32_bf16 v[132:135], v[64:67], v[200:203], v[132:135]\n"
      "ds_read_b128 v[48:51], v220 offset:2048\n"
      "v_exp_f32 v192, v184\n"
      "v_mfma_f32_16x16x32_bf16 v[136:139], v[68:71], v[200:203], v[136:139]\n"
      "ds_read_b128 v[36:39], v221 offset:0\n"
      "v_exp_f32 v193, v185\n"
      "v_mfma_f32_16x16x32_bf16 v[140:143], v[72:75], v[200:203], v[140:143]\n"
      "ds_read_b128 v[52:55], v221 offset:2048\n"
      "v_exp_f32 v194, v186\n"
      "v_mfma_f32_16x16x32_bf16 v[144:147], v[76:79], v[200:203], v[144:147]\n"
      "s_waitcnt lgkmcnt(4)\n"
      "ds_read_b128 v[40:43], v220 offset:4096\n"
      "v_exp_f32 v195, v187\n"
      "v_mfma_f32_16x16x32_bf16 v[148:151], v[80:83], v[200:203], v[148:151]\n"
      "ds_read_b128 v[56:59], v220 offset:6144\n"
      "v_exp_f32 v196, v188\n"
      "v_mfma_f32_16x16x32_bf16 v[152:155], v[84:87], v[200:203], v[152:155]\n"
      "ds_read_b128 v[44:47], v221 offset:4096\n"
      "v_exp_f32 v197, v189\n"
      "v_mfma_f32_16x16x32_bf16 v[156:159], v[88:91], v[200:203], v[156:159]\n"
      "ds_read_b128 v[60:63], v221 offset:6144\n"
      "v_exp_f32 v198, v190\n"
      "v_mfma_f32_16x16x32_bf16 v[160:163], v[92:95], v[200:203], v[160:163]\n"
      ".Lgq_pad2274:\n"
      "s_cbranch_execz .Lgq_pad2322\n"
      "s_waitcnt lgkmcnt(4)\n"
      "buffer_load_dword v12, s[28:31], 0 offen lds\n"
      "s_sub_u32 m0, m0, 4\n"
      "v_exp_f32 v199, v191\n"
      "v_mfma_f32_16x16x32_bf16 v[96:99], v[32:35], v[168:171], v[24:27]\n"
      "s_bitcmp1_b64 s[36:37], 22\n"
      "s_cbranch_scc0 .Lgq_w32\n"
      ".Lgq_r32:\n"
      "ds_read_b64_tr_b16 v[64:65], v210 offset:8192\n"
      "ds_read_b64_tr_b16 v[66:67], v210 offset:10240\n"
      "v_cvt_pk_bf16_f32 v200, v192, v193\n"
      "v_mfma_f32_16x16x32_bf16 v[100:103], v[48:51], v[168:171], v[24:27]\n"
      "ds_read_b64_tr_b16 v[68:69], v211 offset:8192\n"
      "ds_read_b64_tr_b16 v[70:71], v211 offset:10240\n"
      "v_cvt_pk_bf16_f32 v201, v194, v195\n"
      "v_mfma_f32_16x16x32_bf16 v[96:99], v[36:39], v[172:175], v[96:99]\n"
      "ds_read_b64_tr_b16 v[72:73], v212 offset:8192\n"
      "ds_read_b64_tr_b16 v[74:75], v212 offset:10240\n"
      "v_cvt_pk_bf16_f32 v202, v196, v197\n"
      "v_mfma_f32_16x16x32_bf16 v[100:103], v[52:55], v[172:175], v[100:103]\n"
      "s_waitcnt lgkmcnt(6)\n"
      "ds_read_b64_tr_b16 v[76:77], v213 offset:8192\n"
      "ds_read_b64_tr_b16 v[78:79], v213 offset:10240\n"
      "v_cvt_pk_bf16_f32 v203, v198, v199\n"
      "v_mfma_f32_16x16x32_bf16 v[96:99], v[40:43], v[176:179], v[96:99]\n"
      "ds_read_b64_tr_b16 v[80:81], v210 offset:12288\n"
      "ds_read_b64_tr_b16 v[82:83], v210 offset:14336\n"
      "v_or3_b32 v204, v200, v201, v202\n"
      "v_mfma_f32_16x16x32_bf16 v[100:103], v[56:59], v[176:179], v[100:103]\n"
      "ds_read_b64_tr_b16 v[84:85], v211 offset:12288\n"
      "ds_read_b64_tr_b16 v[86:87], v211 offset:14336\n"
      "v_bitop3_b32 v204, v204, v203, s35 bitop3:0xa8\n"
      "v_mfma_f32_16x16x32_bf16 v[96:99], v[44:47], v[180:183], v[96:99]\n"
      "ds_read_b64_tr_b16 v[88:89], v212 offset:12288\n"
      "ds_read_b64_tr_b16 v[90:91], v212 offset:14336\n"
      "v_cmp_ne_u32 vcc, 0, v204\n"
      "v_mfma_f32_16x16x32_bf16 v[100:103], v[60:63], v[180:183], v[100:103]\n"
      "s_cbranch_vccnz .Lgq_rare10\n"
      ".Lgq_rback10:\n"
      "ds_read_b64_tr_b16 v[92:93], v213 offset:12288\n"
      "ds_read_b64_tr_b16 v[94:95], v213 offset:14336\n"
      "v_mfma_f32_16x16x32_bf16 v[164:167], v[28:31], v[200:203], v[164:167]\n"
      "s_waitcnt lgkmcnt(8)\n"
      "ds_read_b128 v[32:35], v220 offset:16384\n"
      "v_mfma_f32_16x16x32_bf16 v[132:135], v[64:67], v[200:203], v[132:135]\n"
      ".Lgq_pad2322:\n"
      "s_cbranch_execz .Lgq_pad2373\n"
      "ds_read_b128 v[48:51], v220 offset:18432\n"
      "v_exp_f32 v192, v96\n"
      "v_mfma_f32_16x16x32_bf16 v[136:139], v[68:71], v[200:203], v[136:139]\n"
      "ds_read_b128 v[36:39], v221 offset:16384\n"
      "v_exp_f32 v193, v97\n"
      "v_mfma_f32_16x16x32_bf16 v[140:143], v[72:75], v[200:203], v[140:143]\n"
      "ds_read_b128 v[52:55], v221 offset:18432\n"
      "v_exp_f32 v194, v98\n"
      "v_mfma_f32_16x16x32_bf16 v[144:147], v[76:79], v[200:203], v[144:147]\n"
      "s_waitcnt lgkmcnt(4)\n"
      "ds_read_b128 v[40:43], v220 offset:20480\n"
      "v_exp_f32 v195, v99\n"
      "v_mfma_f32_16x16x32_bf16 v[148:151], v[80:83], v[200:203], v[148:151]\n"
      "ds_read_b128 v[56:59], v220 offset:22528\n"
      "v_exp_f32 v196, v100\n"
      "v_mfma_f32_16x16x32_bf16 v[152:155], v[84:87], v[200:203], v[152:155]\n"
      "ds_read_b128 v[44:47], v221 offset:20480\n"
      "v_exp_f32 v197, v101\n"
      "v_mfma_f32_16x16x32_bf16 v[156:159], v[88:91], v[200:203], v[156:159]\n"
      "ds_read_b128 v[60:63], v221 offset:22528\n"
      "v_exp_f32 v198, v102\n"
      "v_mfma_f32_16x16x32_bf16 v[160:163], v[92:95], v[200:203], v[160:163]\n"
      "s_waitcnt lgkmcnt(4)\n"
      "v_exp_f32 v199, v103\n"
      "v_mfma_f32_16x16x32_bf16 v[184:187], v[32:35], v[168:171], v[24:27]\n"
      "s_bitcmp1_b64 s[36:37], 23\n"
      "s_cbranch_scc0 .Lgq_w33\n"
      ".Lgq_r33:\n"
      "ds_read_b64_tr_b16 v[64:65], v210 offset:24576\n"
      "ds_read_b64_tr_b16 v[66:67], v210 offset:26624\n"
      "v_cvt_pk_bf16_f32 v200, v192, v193\n"
      "v_mfma_f32_16x16x32_bf16 v[188:191], v[48:51], v[168:171], v[24:27]\n"
      "ds_read_b64_tr_b16 v[68:69], v211 offset:24576\n"
      "ds_read_b64_tr_b16 v[70:71], v211 offset:26624\n"
      "v_cvt_pk_bf16_f32 v201, v194, v195\n"
      "v_mfma_f32_16x16x32_bf16 v[184:187], v[36:39], v[172:175], v[184:187]\n"
      "ds_read_b64_tr_b16 v[72:73], v212 offset:24576\n"
      "ds_read_b64_tr_b16 v[74:75], v212 offset:26624\n"
      "v_cvt_pk_bf16_f32 v202, v196, v197\n"
      "v_mfma_f32_16x16x32_bf16 v[188:191], v[52:55], v[172:175], v[188:191]\n"
      "s_waitcnt lgkmcnt(6)\n"
      "ds_read_b64_tr_b16 v[76:77], v213 offset:24576\n"
      "ds_read_b64_tr_b16 v[78:79], v213 offset:26624\n"
      "v_cvt_pk_bf16_f32 v203, v198, v199\n"
      "v_mfma_f32_16x16x32_bf16 v[184:187], v[40:43], v[176:179], v[184:187]\n"
      "ds_read_b64_tr_b16 v[80:81], v210 offset:28672\n"
      "ds_read_b64_tr_b16 v[82:83], v210 offset:30720\n"
      "v_or3_b32 v204, v200, v201, v202\n"
      "v_mfma_f32_16x16x32_bf16 v[188:191], v[56:59], v[176:179], v[188:191]\n"
      ".Lgq_pad2373:\n"
      "s_cbranch_execz .Lgq_pad2424\n"
      "ds_read_b64_tr_b16 v[84:85], v211 offset:28672\n"
      "ds_read_b64_tr_b16 v[86:87], v211 offset:30720\n"
      "v_bitop3_b32 v204, v204, v203, s35 bitop3:0xa8\n"
      "v_mfma_f32_16x16x32_bf16 v[184:187], v[44:47], v[180:183], v[184:187]\n"
      "ds_read_b64_tr_b16 v[88:89], v212 offset:28672\n"
      "ds_read_b64_tr_b16 v[90:91], v212 offset:30720\n"
      "v_cmp_ne_u32 vcc, 0, v204\n"
      "v_mfma_f32_16x16x32_bf16 v[188:191], v[60:63], v[180:183], v[188:191]\n"
      "s_cbranch_vccnz .Lgq_rare11\n"
      ".Lgq_rback11:\n"
      "ds_read_b64_tr_b16 v[92:93], v213 offset:28672\n"
      "ds_read_b64_tr_b16 v[94:95], v213 offset:30720\n"
      "v_mfma_f32_16x16x32_bf16 v[164:167], v[28:31], v[200:203], v[164:167]\n"
      "s_waitcnt lgkmcnt(8)\n"
      "ds_read_b128 v[32:35], v220 offset:32768\n"
      "v_mfma_f32_16x16x32_bf16 v[132:135], v[64:67], v[200:203], v[132:135]\n"
      "ds_read_b128 v[48:51], v220 offset:34816\n"
      "v_exp_f32 v192, v184\n"
      "v_mfma_f32_16x16x32_bf16 v[136:139], v[68:71], v[200:203], v[136:139]\n"
      "ds_read_b128 v[36:39], v221 offset:32768\n"
      "v_exp_f32 v193, v185\n"
      "v_mfma_f32_16x16x32_bf16 v[140:143], v[72:75], v[200:203], v[140:143]\n"
      "ds_read_b128 v[52:55], v221 offset:34816\n"
      "v_exp_f32 v194, v186\n"
      "v_mfma_f32_16x16x32_bf16 v[144:147], v[76:79], v[200:203], v[144:147]\n"
      "s_waitcnt lgkmcnt(4)\n"
      "ds_read_b128 v[40:43], v220 offset:36864\n"
      "v_exp_f32 v195, v187\n"
      "v_mfma_f32_16x16x32_bf16 v[148:151], v[80:83], v[200:203], v[148:151]\n"
      "ds_read_b128 v[56:59], v220 offset:38912\n"
      "v_exp_f32 v196, v188\n"
      "v_mfma_f32_16x16x32_bf16 v[152:155], v[84:87], v[200:203], v[152:155]\n"
      "ds_read_b128 v[44:47], v221 offset:36864\n"
      "v_exp_f32 v197, v189\n"
      "v_mfma_f32_16x16x32_bf16 v[156:159], v[88:91], v[200:203], v[156:159]\n"
      "ds_read_b128 v[60:63], v221 offset:38912\n"
      "v_exp_f32 v198, v190\n"
      "v_mfma_f32_16x16x32_bf16 v[160:163], v[92:95], v[200:203], v[160:163]\n"
      "s_waitcnt lgkmcnt(4)\n"
      "v_exp_f32 v199, v191\n"
      "v_mfma_f32_16x16x32_bf16 v[96:99], v[32:35], v[168:171], v[24:27]\n"
      "s_bitcmp1_b64 s[36:37], 24\n"
      "s_cbranch_scc0 .Lgq_w34\n"
      ".Lgq_r34:\n"
      "ds_read_b64_tr_b16 v[64:65], v210 offset:40960\n"
      "ds_read_b64_tr_b16 v[66:67], v210 offset:43008\n"
      "v_cvt_pk_bf16_f32 v200, v192, v193\n"
      "v_mfma_f32_16x16x32_bf16 v[100:103], v[48:51], v[168:171], v[24:27]\n"
      ".Lgq_pad2424:\n"
      "s_cbranch_execz .Lgq_pad2472\n"
      "ds_read_b64_tr_b16 v[68:69], v211 offset:40960\n"
      "ds_read_b64_tr_b16 v[70:71], v211 offset:43008\n"
      "v_cvt_pk_bf16_f32 v201, v194, v195\n"
      "v_mfma_f32_16x16x32_bf16 v[96:99], v[36:39], v[172:175], v[96:99]\n"
      "ds_read_b64_tr_b16 v[72:73], v212 offset:40960\n"
      "ds_read_b64_tr_b16 v[74:75], v212 offset:43008\n"
      "v_cvt_pk_bf16_f32 v202, v196, v197\n"
      "v_mfma_f32_16x16x32_bf16 v[100:103], v[52:55], v[172:175], v[100:103]\n"
      "s_waitcnt lgkmcnt(6)\n"
      "ds_read_b64_tr_b16 v[76:77], v213 offset:40960\n"
      "ds_read_b64_tr_b16 v[78:79], v213 offset:43008\n"
      "v_cvt_pk_bf16_f32 v203, v198, v199\n"
      "v_mfma_f32_16x16x32_bf16 v[96:99], v[40:43], v[176:179], v[96:99]\n"
      "ds_read_b64_tr_b16 v[80:81], v210 offset:45056\n"
      "ds_read_b64_tr_b16 v[82:83], v210 offset:47104\n"
      "v_or3_b32 v204, v200, v201, v202\n"
      "v_mfma_f32_16x16x32_bf16 v[100:103], v[56:59], v[176:179], v[100:103]\n"
      "ds_read_b64_tr_b16 v[84:85], v211 offset:45056\n"
      "ds_read_b64_tr_b16 v[86:87], v211 offset:47104\n"
      "v_bitop3_b32 v204, v204, v203, s35 bitop3:0xa8\n"
      "v_mfma_f32_16x16x32_bf16 v[96:99], v[44:47], v[180:183], v[96:99]\n"
      "ds_read_b64_tr_b16 v[88:89], v212 offset:45056\n"
      "ds_read_b64_tr_b16 v[90:91], v212 offset:47104\n"
      "v_cmp_ne_u32 vcc, 0, v204\n"
      "v_mfma_f32_16x16x32_bf16 v[100:103], v[60:63], v[180:183], v[100:103]\n"
      "s_cbranch_vccnz .Lgq_rare12\n"
      ".Lgq_rback12:\n"
      "ds_read_b64_tr_b16 v[92:93], v213 offset:45056\n"
      "ds_read_b64_tr_b16 v[94:95], v213 offset:47104\n"
      "v_mfma_f32_16x16x32_bf16 v[164:167], v[28:31], v[200:203], v[164:167]\n"
      "s_waitcnt lgkmcnt(8)\n"
      "ds_read_b128 v[32:35], v220 offset:49152\n"
      "v_mfma_f32_16x16x32_bf16 v[132:135], v[64:67], v[200:203], v[132:135]\n"
      "ds_read_b128 v[48:51], v220 offset:51200\n"
      "v_exp_f32 v192, v96\n"
      "v_mfma_f32_16x16x32_bf16 v[136:139], v[68:71], v[200:203], v[136:139]\n"
      "ds_read_b128 v[36:39], v221 offset:49152\n"
      "v_exp_f32 v193, v97\n"
      "v_mfma_f32_16x16x32_bf16 v[140:143], v[72:75], v[200:203], v[140:143]\n"
      "ds_read_b128 v[52:55], v221 offset:51200\n"
      "v_exp_f32 v194, v98\n"
      "v_mfma_f32_16x16x32_bf16 v[144:147], v[76:79], v[200:203], v[144:147]\n"
      "s_waitcnt lgkmcnt(4)\n"
      "ds_read_b128 v[40:43], v220 offset:53248\n"
      "v_exp_f32 v195, v99\n"
      "v_mfma_f32_16x16x32_bf16 v[148:151], v[80:83], v[200:203], v[148:151]\n"
      ".Lgq_pad2472:\n"
      "s_cbranch_execz .Lgq_pad2484\n"
      "ds_read_b128 v[56:59], v220 offset:55296\n"
      "v_exp_f32 v196, v100\n"
      "v_mfma_f32_16x16x32_bf16 v[152:155], v[84:87], v[200:203], v[152:155]\n"
      "ds_read_b128 v[44:47], v221 offset:53248\n"
      "v_exp_f32 v197, v101\n"
      "v_mfma_f32_16x16x32_bf16 v[156:159], v[88:91], v[200:203], v[156:159]\n"
      "ds_read_b128 v[60:63], v221 offset:55296\n"
      "v_exp_f32 v198, v102\n"
      "v_mfma_f32_16x16x32_bf16 v[160:163], v[92:95], v[200:203], v[160:163]\n"
      "s_waitcnt lgkmcnt(4)\n"
      "v_exp_f32 v199, v103\n"
      ".Lgq_pad2484:\n"
      "s_cbranch_execz .Lgq_pad2541\n"
      "v_mfma_f32_16x16x32_bf16 v[184:187], v[32:35], v[168:171], v[24:27]\n"
      "s_bitcmp1_b64 s[36:37], 25\n"
      "s_cbranch_scc0 .Lgq_w35\n"
      ".Lgq_r35:\n"
      "ds_read_b64_tr_b16 v[64:65], v210 offset:57344\n"
      "ds_read_b64_tr_b16 v[66:67], v210 offset:59392\n"
      "v_cvt_pk_bf16_f32 v200, v192, v193\n"
      "v_mfma_f32_16x16x32_bf16 v[188:191], v[48:51], v[168:171], v[24:27]\n"
      "ds_read_b64_tr_b16 v[68:69], v211 offset:57344\n"
      "ds_read_b64_tr_b16 v[70:71], v211 offset:59392\n"
      "v_cvt_pk_bf16_f32 v201, v194, v195\n"
      "v_mfma_f32_16x16x32_bf16 v[184:187], v[36:39], v[172:175], v[184:187]\n"
      "ds_read_b64_tr_b16 v[72:73], v212 offset:57344\n"
      "ds_read_b64_tr_b16 v[74:75], v212 offset:59392\n"
      "v_cvt_pk_bf16_f32 v202, v196, v197\n"
      "v_mfma_f32_16x16x32_bf16 v[188:191], v[52:55], v[172:175], v[188:191]\n"
      "s_waitcnt lgkmcnt(6)\n"
      "ds_read_b64_tr_b16 v[76:77], v213 offset:57344\n"
      "ds_read_b64_tr_b16 v[78:79], v213 offset:59392\n"
      "v_cvt_pk_bf16_f32 v203, v198, v199\n"
      "v_mfma_f32_16x16x32_bf16 v[184:187], v[40:43], v[176:179], v[184:187]\n"
      "ds_read_b64_tr_b16 v[80:81], v210 offset:61440\n"
      "ds_read_b64_tr_b16 v[82:83], v210 offset:63488\n"
      "v_or3_b32 v204, v200, v201, v202\n"
      "v_mfma_f32_16x16x32_bf16 v[188:191], v[56:59], v[176:179], v[188:191]\n"
      "ds_read_b64_tr_b16 v[84:85], v211 offset:61440\n"
      "ds_read_b64_tr_b16 v[86:87], v211 offset:63488\n"
      "v_bitop3_b32 v204, v204, v203, s35 bitop3:0xa8\n"
      "v_mfma_f32_16x16x32_bf16 v[184:187], v[44:47], v[180:183], v[184:187]\n"
      "ds_read_b64_tr_b16 v[88:89], v212 offset:61440\n"
      "ds_read_b64_tr_b16 v[90:91], v212 offset:63488\n"
      "v_cmp_ne_u32 vcc, 0, v204\n"
      "v_mfma_f32_16x16x32_bf16 v[188:191], v[60:63], v[180:183], v[188:191]\n"
      "s_cbranch_vccnz .Lgq_rare13\n"
      ".Lgq_rback13:\n"
      "ds_read_b64_tr_b16 v[92:93], v213 offset:61440\n"
      "ds_read_b64_tr_b16 v[94:95], v213 offset:63488\n"
      "v_mfma_f32_16x16x32_bf16 v[164:167], v[28:31], v[200:203], v[164:167]\n"
      "s_waitcnt lgkmcnt(8)\n"
      "ds_read_b128 v[32:35], v222 offset:0\n"
      "v_mfma_f32_16x16x32_bf16 v[132:135], v[64:67], v[200:203], v[132:135]\n"
      "ds_read_b128 v[48:51], v222 offset:2048\n"
      "v_exp_f32 v192, v184\n"
      "v_mfma_f32_16x16x32_bf16 v[136:139], v[68:71], v[200:203], v[136:139]\n"
      "ds_read_b128 v[36:39], v223 offset:0\n"
      "v_exp_f32 v193, v185\n"
      "v_mfma_f32_16x16x32_bf16 v[140:143], v[72:75], v[200:203], v[140:143]\n"
      "ds_read_b128 v[52:55], v223 offset:2048\n"
      "v_exp_f32 v194, v186\n"
      "v_mfma_f32_16x16x32_bf16 v[144:147], v[76:79], v[200:203], v[144:147]\n"
      "s_waitcnt lgkmcnt(4)\n"
      "ds_read_b128 v[40:43], v222 offset:4096\n"
      "v_exp_f32 v195, v187\n"
      "v_mfma_f32_16x16x32_bf16 v[148:151], v[80:83], v[200:203], v[148:151]\n"
      ".Lgq_pad2541:\n"
      "s_cbranch_execz .Lgq_pad2562\n"
      "ds_read_b128 v[56:59], v222 offset:6144\n"
      "v_exp_f32 v196, v188\n"
      "v_mfma_f32_16x16x32_bf16 v[152:155], v[84:87], v[200:203], v[152:155]\n"
      "ds_read_b128 v[44:47], v223 offset:4096\n"
      "v_exp_f32 v197, v189\n"
      "v_mfma_f32_16x16x32_bf16 v[156:159], v[88:91], v[200:203], v[156:159]\n"
      "ds_read_b128 v[60:63], v223 offset:6144\n"
      "v_exp_f32 v198, v190\n"
      "v_mfma_f32_16x16x32_bf16 v[160:163], v[92:95], v[200:203], v[160:163]\n"
      "s_waitcnt lgkmcnt(4)\n"
      "v_exp_f32 v199, v191\n"
      "v_mfma_f32_16x16x32_bf16 v[96:99], v[32:35], v[168:171], v[24:27]\n"
      "s_bitcmp1_b64 s[36:37], 26\n"
      "s_cbranch_scc0 .Lgq_w36\n"
      ".Lgq_r36:\n"
      "ds_read_b64_tr_b16 v[64:65], v214 offset:8192\n"
      "ds_read_b64_tr_b16 v[66:67], v214 offset:10240\n"
      "v_cvt_pk_bf16_f32 v200, v192, v193\n"
      "v_mfma_f32_16x16x32_bf16 v[100:103], v[48:51], v[168:171], v[24:27]\n"
      ".Lgq_pad2562:\n"
      "s_cbranch_execz .Lgq_walked\n"
      "ds_read_b64_tr_b16 v[68:69], v215 offset:8192\n"
      "ds_read_b64_tr_b16 v[70:71], v215 offset:10240\n"
      "v_cvt_pk_bf16_f32 v201, v194, v195\n"
      "v_mfma_f32_16x16x32_bf16 v[96:99], v[36:39], v[172:175], v[96:99]\n"
      "ds_read_b64_tr_b16 v[72:73], v216 offset:8192\n"
      "ds_read_b64_tr_b16 v[74:75], v216 offset:10240\n"
      "v_cvt_pk_bf16_f32 v202, v196, v197\n"
      "v_mfma_f32_16x16x32_bf16 v[100:103], v[52:55], v[172:175], v[100:103]\n"
      "s_waitcnt lgkmcnt(6)\n"
      "ds_read_b64_tr_b16 v[76:77], v217 offset:8192\n"
      "ds_read_b64_tr_b16 v[78:79], v217 offset:10240\n"
      "v_cvt_pk_bf16_f32 v203, v198, v199\n"
      "v_mfma_f32_16x16x32_bf16 v[96:99], v[40:43], v[176:179], v[96:99]\n"
      "ds_read_b64_tr_b16 v[80:81], v214 offset:12288\n"
      "ds_read_b64_tr_b16 v[82:83], v214 offset:14336\n"
      "v_or3_b32 v204, v200, v201, v202\n"
      "v_mfma_f32_16x16x32_bf16 v[100:103], v[56:59], v[176:179], v[100:103]\n"
      "ds_read_b64_tr_b16 v[84:85], v215 offset:12288\n"
      "ds_read_b64_tr_b16 v[86:87], v215 offset:14336\n"
      "v_bitop3_b32 v204, v204, v203, s35 bitop3:0xa8\n"
      "v_mfma_f32_16x16x32_bf16 v[96:99], v[44:47], v[180:183], v[96:99]\n"
      "ds_read_b64_tr_b16 v[88:89], v216 offset:12288\n"
      "ds_read_b64_tr_b16 v[90:91], v216 offset:14336\n"
      "v_cmp_ne_u32 vcc, 0, v204\n"
      "v_mfma_f32_16x16x32_bf16 v[100:103], v[60:63], v[180:183], v[100:103]\n"
      "s_cbranch_vccnz .Lgq_rare14\n"
      ".Lgq_rback14:\n"
      "ds_read_b64_tr_b16 v[92:93], v217 offset:12288\n"
      "ds_read_b64_tr_b16 v[94:95], v217 offset:14336\n"
      "v_mfma_f32_16x16x32_bf16 v[164:167], v[28:31], v[200:203], v[164:167]\n"
      "s_waitcnt lgkmcnt(8)\n"
      "s_bitcmp1_b64 s[36:37], 27\n"
      "s_cbranch_scc0 .Lgq_w37\n"
      ".Lgq_r37:\n"
      "ds_read_b64_tr_b16 v[32:33], v214 offset:16384\n"
      "ds_read_b64_tr_b16 v[34:35], v214 offset:18432\n"
      "v_mfma_f32_16x16x32_bf16 v[132:135], v[64:67], v[200:203], v[132:135]\n"
      "ds_read_b64_tr_b16 v[36:37], v215 offset:16384\n"
      "ds_read_b64_tr_b16 v[38:39], v215 offset:18432\n"
      "v_exp_f32 v192, v96\n"
      "v_mfma_f32_16x16x32_bf16 v[136:139], v[68:71], v[200:203], v[136:139]\n"
      "ds_read_b64_tr_b16 v[40:41], v216 offset:16384\n"
      "ds_read_b64_tr_b16 v[42:43], v216 offset:18432\n"
      "v_exp_f32 v193, v97\n"
      "v_mfma_f32_16x16x32_bf16 v[140:143], v[72:75], v[200:203], v[140:143]\n"
      ".Lgq_pad2609:\n"
      "s_cbranch_execz .Lgq_pad2653\n"
      "ds_read_b64_tr_b16 v[44:45], v217 offset:16384\n"
      "ds_read_b64_tr_b16 v[46:47], v217 offset:18432\n"
      "v_exp_f32 v194, v98\n"
      "v_mfma_f32_16x16x32_bf16 v[144:147], v[76:79], v[200:203], v[144:147]\n"
      "s_waitcnt lgkmcnt(8)\n"
      "ds_read_b64_tr_b16 v[48:49], v214 offset:20480\n"
      "ds_read_b64_tr_b16 v[50:51], v214 offset:22528\n"
      "v_exp_f32 v195, v99\n"
      "v_mfma_f32_16x16x32_bf16 v[148:151], v[80:83], v[200:203], v[148:151]\n"
      "ds_read_b64_tr_b16 v[52:53], v215 offset:20480\n"
      "ds_read_b64_tr_b16 v[54:55], v215 offset:22528\n"
      "v_exp_f32 v196, v100\n"
      "v_mfma_f32_16x16x32_bf16 v[152:155], v[84:87], v[200:203], v[152:155]\n"
      "ds_read_b64_tr_b16 v[56:57], v216 offset:20480\n"
      "ds_read_b64_tr_b16 v[58:59], v216 offset:22528\n"
      "v_exp_f32 v197, v101\n"
      "v_mfma_f32_16x16x32_bf16 v[156:159], v[88:91], v[200:203], v[156:159]\n"
      "ds_read_b64_tr_b16 v[60:61], v217 offset:20480\n"
      "ds_read_b64_tr_b16 v[62:63], v217 offset:22528\n"
      "v_exp_f32 v198, v102\n"
      "v_mfma_f32_16x16x32_bf16 v[160:163], v[92:95], v[200:203], v[160:163]\n"
      "v_exp_f32 v199, v103\n"
      "v_cvt_pk_bf16_f32 v200, v192, v193\n"
      "v_cvt_pk_bf16_f32 v201, v194, v195\n"
      "v_cvt_pk_bf16_f32 v202, v196, v197\n"
      "v_cvt_pk_bf16_f32 v203, v198, v199\n"
      "v_or3_b32 v204, v200, v201, v202\n"
      "v_bitop3_b32 v204, v204, v203, s35 bitop3:0xa8\n"
      "v_cmp_ne_u32 vcc, 0, v204\n"
      "s_cbranch_vccnz .Lgq_rare15\n"
      ".Lgq_rback15:\n"
      "v_mfma_f32_16x16x32_bf16 v[164:167], v[28:31], v[200:203], v[164:167]\n"
      "s_waitcnt lgkmcnt(0)\n"
      "v_mfma_f32_16x16x32_bf16 v[132:135], v[32:35], v[200:203], v[132:135]\n"
      "v_mfma_f32_16x16x32_bf16 v[136:139], v[36:39], v[200:203], v[136:139]\n"
      "v_mfma_f32_16x16x32_bf16 v[140:143], v[40:43], v[200:203], v[140:143]\n"
      "v_mfma_f32_16x16x32_bf16 v[144:147], v[44:47], v[200:203], v[144:147]\n"
      "v_mfma_f32_16x16x32_bf16 v[148:151], v[48:51], v[200:203], v[148:151]\n"
      "v_mfma_f32_16x16x32_bf16 v[152:155], v[52:55], v[200:203], v[152:155]\n"
      "v_mfma_f32_16x16x32_bf16 v[156:159], v[56:59], v[200:203], v[156:159]\n"
      "v_rcp_f32 v204, v164\n"
      "v_mfma_f32_16x16x32_bf16 v[160:163], v[60:63], v[200:203], v[160:163]\n"
      ".Lgq_pad2653:\n"
      "s_cbranch_execz .Lgq_pad2701\n"
      "s_nop 0\n"
      "v_pk_mul_f32 v[132:133], v[132:133], v[204:205] op_sel_hi:[1,0]\n"
      "v_pk_mul_f32 v[134:135], v[134:135], v[204:205] op_sel_hi:[1,0]\n"
      "v_pk_mul_f32 v[136:137], v[136:137], v[204:205] op_sel_hi:[1,0]\n"
      "v_pk_mul_f32 v[138:139], v[138:139], v[204:205] op_sel_hi:[1,0]\n"
      "v_cvt_pk_bf16_f32 v168, v132, v133\n"
      "v_cvt_pk_bf16_f32 v169, v134, v135\n"
      "v_cvt_pk_bf16_f32 v170, v136, v137\n"
      "v_cvt_pk_bf16_f32 v171, v138, v139\n"
      "v_add_u32 v168, s24, v168\n"
      "buffer_store_dwordx4 v[168:171], v4, s[28:31], s44 offen offset:0 sc1\n"
      "v_add3_u32 v8, s24, v168, v170\n"
      "v_add3_u32 v9, s25, v169, v171\n"
      "v_pk_mul_f32 v[140:141], v[140:141], v[204:205] op_sel_hi:[1,0]\n"
      "v_pk_mul_f32 v[142:143], v[142:143], v[204:205] op_sel_hi:[1,0]\n"
      "v_pk_mul_f32 v[144:145], v[144:145], v[204:205] op_sel_hi:[1,0]\n"
      "v_pk_mul_f32 v[146:147], v[146:147], v[204:205] op_sel_hi:[1,0]\n"
      "v_cvt_pk_bf16_f32 v172, v140, v141\n"
      "v_cvt_pk_bf16_f32 v173, v142, v143\n"
      "v_cvt_pk_bf16_f32 v174, v144, v145\n"
      "v_cvt_pk_bf16_f32 v175, v146, v147\n"
      "v_add_u32 v172, s24, v172\n"
      "buffer_store_dwordx4 v[172:175], v4, s[28:31], s44 offen offset:1024 sc1\n"
      "v_add3_u32 v8, v8, v172, v174\n"
      "v_add3_u32 v9, v9, v173, v175\n"
      "v_pk_mul_f32 v[148:149], v[148:149], v[204:205] op_sel_hi:[1,0]\n"
      "v_pk_mul_f32 v[150:151], v[150:151], v[204:205] op_sel_hi:[1,0]\n"
      "v_pk_mul_f32 v[152:153], v[152:153], v[204:205] op_sel_hi:[1,0]\n"
      "v_pk_mul_f32 v[154:155], v[154:155], v[204:205] op_sel_hi:[1,0]\n"
      "v_cvt_pk_bf16_f32 v176, v148, v149\n"
      "v_cvt_pk_bf16_f32 v177, v150, v151\n"
      "v_cvt_pk_bf16_f32 v178, v152, v153\n"
      "v_cvt_pk_bf16_f32 v179, v154, v155\n"
      "v_add_u32 v176, s24, v176\n"
      "buffer_store_dwordx4 v[176:179], v4, s[28:31], s44 offen offset:2048 sc1\n"
      "v_add3_u32 v8, v8, v176, v178\n"
      "v_add3_u32 v9, v9, v177, v179\n"
      "v_pk_mul_f32 v[156:157], v[156:157], v[204:205] op_sel_hi:[1,0]\n"
      "v_pk_mul_f32 v[158:159], v[158:159], v[204:205] op_sel_hi:[1,0]\n"
      "v_pk_mul_f32 v[160:161], v[160:161], v[204:205] op_sel_hi:[1,0]\n"
      "v_pk_mul_f32 v[162:163], v[162:163], v[204:205] op_sel_hi:[1,0]\n"
      "v_cvt_pk_bf16_f32 v180, v156, v157\n"
      "v_cvt_pk_bf16_f32 v181, v158, v159\n"
      "v_cvt_pk_bf16_f32 v182, v160, v161\n"
      "v_cvt_pk_bf16_f32 v183, v162, v163\n"
      "v_add_u32 v180, s24, v180\n"
      "buffer_store_dwordx4 v[180:183], v4, s[28:31], s44 offen offset:3072 sc1\n"
      ".Lgq_pad2701:\n"
      "s_cbranch_execz .Lgq_pad2747\n"
      "v_add3_u32 v8, v8, v180, v182\n"
      "v_add3_u32 v9, v9, v181, v183\n"
      "buffer_store_dwordx2 v[8:9], v5, s[28:31], s44 offen sc1\n"
      "v_mov_b32 v14, 0\n"
      "ds_write_b32 v11, v14 offset:192\n"
      "v_subrev_u32 v168, s24, v168\n"
      "v_subrev_u32 v172, s24, v172\n"
      "v_subrev_u32 v176, s24, v176\n"
      "v_subrev_u32 v180, s24, v180\n"
      "s_bitcmp1_b64 s[36:37], 29\n"
      "s_cbranch_scc0 .Lgq_w38\n"
      ".Lgq_r38:\n"
      "ds_read_b64_tr_b16 v[16:17], v232 offset:0\n"
      "ds_read_b64_tr_b16 v[18:19], v232 offset:4608\n"
      "ds_read_b64_tr_b16 v[20:21], v232 offset:32\n"
      "ds_read_b64_tr_b16 v[22:23], v232 offset:4640\n"
      "ds_read_b64_tr_b16 v[24:25], v232 offset:64\n"
      "ds_read_b64_tr_b16 v[26:27], v232 offset:4672\n"
      "ds_read_b64_tr_b16 v[28:29], v232 offset:96\n"
      "ds_read_b64_tr_b16 v[30:31], v232 offset:4704\n"
      "ds_read_b64_tr_b16 v[32:33], v232 offset:128\n"
      "ds_read_b64_tr_b16 v[34:35], v232 offset:4736\n"
      "ds_read_b64_tr_b16 v[36:37], v232 offset:160\n"
      "ds_read_b64_tr_b16 v[38:39], v232 offset:4768\n"
      "ds_read_b64_tr_b16 v[40:41], v232 offset:192\n"
      "ds_read_b64_tr_b16 v[42:43], v232 offset:4800\n"
      "ds_read_b64_tr_b16 v[44:45], v232 offset:224\n"
      "ds_read_b64_tr_b16 v[46:47], v232 offset:4832\n"
      "ds_read_b64_tr_b16 v[48:49], v232 offset:256\n"
      "ds_read_b64_tr_b16 v[50:51], v232 offset:4864\n"
      "ds_read_b64_tr_b16 v[52:53], v232 offset:9216\n"
      "ds_read_b64_tr_b16 v[54:55], v232 offset:13824\n"
      "ds_read_b64_tr_b16 v[56:57], v232 offset:9248\n"
      "ds_read_b64_tr_b16 v[58:59], v232 offset:13856\n"
      "ds_read_b64_tr_b16 v[60:61], v232 offset:9280\n"
      "ds_read_b64_tr_b16 v[62:63], v232 offset:13888\n"
      "ds_read_b64_tr_b16 v[64:65], v232 offset:9312\n"
      "ds_read_b64_tr_b16 v[66:67], v232 offset:13920\n"
      "ds_read_b64_tr_b16 v[68:69], v232 offset:9344\n"
      "ds_read_b64_tr_b16 v[70:71], v232 offset:13952\n"
      "ds_read_b64_tr_b16 v[72:73], v232 offset:9376\n"
      "ds_read_b64_tr_b16 v[74:75], v232 offset:13984\n"
      "ds_read_b64_tr_b16 v[76:77], v232 offset:9408\n"
      "ds_read_b64_tr_b16 v[78:79], v232 offset:14016\n"
      ".Lgq_pad2747:\n"
      "s_cbranch_execz .Lgq_pad2788\n"
      "ds_read_b64_tr_b16 v[80:81], v232 offset:9440\n"
      "ds_read_b64_tr_b16 v[82:83], v232 offset:14048\n"
      "ds_read_b64_tr_b16 v[84:85], v232 offset:9472\n"
      "ds_read_b64_tr_b16 v[86:87], v232 offset:14080\n"
      "s_bitcmp1_b64 s[36:37], 31\n"
      "s_cbranch_scc0 .Lgq_w39\n"
      ".Lgq_r39:\n"
      "ds_read_b64_tr_b16 v[88:89], v232 offset:18432\n"
      "ds_read_b64_tr_b16 v[90:91], v232 offset:23040\n"
      "ds_read_b64_tr_b16 v[92:93], v232 offset:18464\n"
      "ds_read_b64_tr_b16 v[94:95], v232 offset:23072\n"
      "ds_read_b64_tr_b16 v[132:133], v232 offset:18496\n"
      "ds_read_b64_tr_b16 v[134:135], v232 offset:23104\n"
      "ds_read_b64_tr_b16 v[136:137], v232 offset:18528\n"
      "ds_read_b64_tr_b16 v[138:139], v232 offset:23136\n"
      "ds_read_b64_tr_b16 v[140:141], v232 offset:18560\n"
      "ds_read_b64_tr_b16 v[142:143], v232 offset:23168\n"
      "ds_read_b64_tr_b16 v[144:145], v232 offset:18592\n"
      "ds_read_b64_tr_b16 v[146:147], v232 offset:23200\n"
      "ds_read_b64_tr_b16 v[148:149], v232 offset:18624\n"
      "ds_read_b64_tr_b16 v[150:151], v232 offset:23232\n"
      "ds_read_b64_tr_b16 v[152:153], v232 offset:18656\n"
      "ds_read_b64_tr_b16 v[154:155], v232 offset:23264\n"
      "ds_read_b64_tr_b16 v[156:157], v232 offset:18688\n"
      "ds_read_b64_tr_b16 v[158:159], v232 offset:23296\n"
      "ds_read_b64_tr_b16 v[160:161], v232 offset:27648\n"
      "ds_read_b64_tr_b16 v[162:163], v232 offset:32256\n"
      "ds_read_b64_tr_b16 v[164:165], v232 offset:27680\n"
      "ds_read_b64_tr_b16 v[166:167], v232 offset:32288\n"
      "ds_read_b64_tr_b16 v[244:245], v232 offset:27712\n"
      "ds_read_b64_tr_b16 v[246:247], v232 offset:32320\n"
      "ds_read_b64_tr_b16 v[248:249], v232 offset:27744\n"
      "ds_read_b64_tr_b16 v[250:251], v232 offset:32352\n"
      "ds_read_b64_tr_b16 v[252:253], v232 offset:27776\n"
      "ds_read_b64_tr_b16 v[254:255], v232 offset:32384\n"
      "s_waitcnt lgkmcnt(14)\n"
      "v_mfma_f32_16x16x32_bf16 v[96:99], v[16:19], v[168:171], 0\n"
      "ds_read_b64_tr_b16 v[16:17], v232 offset:27808\n"
      "ds_read_b64_tr_b16 v[18:19], v232 offset:32416\n"
      "v_mfma_f32_16x16x32_bf16 v[100:103], v[20:23], v[168:171], 0\n"
      ".Lgq_pad2788:\n"
      "s_cbranch_execz .Lgq_pad2833\n"
      "ds_read_b64_tr_b16 v[20:21], v232 offset:27840\n"
      "ds_read_b64_tr_b16 v[22:23], v232 offset:32448\n"
      "v_mfma_f32_16x16x32_bf16 v[104:107], v[24:27], v[168:171], 0\n"
      "ds_read_b64_tr_b16 v[24:25], v232 offset:27872\n"
      "ds_read_b64_tr_b16 v[26:27], v232 offset:32480\n"
      "v_mfma_f32_16x16x32_bf16 v[108:111], v[28:31], v[168:171], 0\n"
      "ds_read_b64_tr_b16 v[28:29], v232 offset:27904\n"
      "ds_read_b64_tr_b16 v[30:31], v232 offset:32512\n"
      "v_mfma_f32_16x16x32_bf16 v[112:115], v[32:35], v[168:171], 0\n"
      "s_bitcmp1_b64 s[36:37], 33\n"
      "s_cbranch_scc0 .Lgq_w40\n"
      ".Lgq_r40:\n"
      "ds_read_b64_tr_b16 v[32:33], v232 offset:36864\n"
      "ds_read_b64_tr_b16 v[34:35], v232 offset:41472\n"
      "v_mfma_f32_16x16x32_bf16 v[116:119], v[36:39], v[168:171], 0\n"
      "ds_read_b64_tr_b16 v[36:37], v232 offset:36896\n"
      "ds_read_b64_tr_b16 v[38:39], v232 offset:41504\n"
      "v_mfma_f32_16x16x32_bf16 v[120:123], v[40:43], v[168:171], 0\n"
      "ds_read_b64_tr_b16 v[40:41], v232 offset:36928\n"
      "ds_read_b64_tr_b16 v[42:43], v232 offset:41536\n"
      "v_mfma_f32_16x16x32_bf16 v[124:127], v[44:47], v[168:171], 0\n"
      "ds_read_b64_tr_b16 v[44:45], v232 offset:36960\n"
      "ds_read_b64_tr_b16 v[46:47], v232 offset:41568\n"
      "v_mfma_f32_16x16x32_bf16 v[128:131], v[48:51], v[168:171], 0\n"
      "ds_read_b64_tr_b16 v[48:49], v232 offset:36992\n"
      "ds_read_b64_tr_b16 v[50:51], v232 offset:41600\n"
      "ds_read_b64_tr_b16 v[168:169], v232 offset:37024\n"
      "ds_read_b64_tr_b16 v[170:171], v232 offset:41632\n"
      "v_mfma_f32_16x16x32_bf16 v[96:99], v[52:55], v[172:175], v[96:99]\n"
      "ds_read_b64_tr_b16 v[52:53], v232 offset:37056\n"
      "ds_read_b64_tr_b16 v[54:55], v232 offset:41664\n"
      "v_mfma_f32_16x16x32_bf16 v[100:103], v[56:59], v[172:175], v[100:103]\n"
      "ds_read_b64_tr_b16 v[56:57], v232 offset:37088\n"
      "ds_read_b64_tr_b16 v[58:59], v232 offset:41696\n"
      "v_mfma_f32_16x16x32_bf16 v[104:107], v[60:63], v[172:175], v[104:107]\n"
      "ds_read_b64_tr_b16 v[60:61], v232 offset:37120\n"
      "ds_read_b64_tr_b16 v[62:63], v232 offset:41728\n"
      "v_mfma_f32_16x16x32_bf16 v[108:111], v[64:67], v[172:175], v[108:111]\n"
      "ds_read_b64_tr_b16 v[64:65], v232 offset:46080\n"
      "ds_read_b64_tr_b16 v[66:67], v232 offset:50688\n"
      "v_mfma_f32_16x16x32_bf16 v[112:115], v[68:71], v[172:175], v[112:115]\n"
      "ds_read_b64_tr_b16 v[68:69], v232 offset:46112\n"
      "ds_read_b64_tr_b16 v[70:71], v232 offset:50720\n"
      "v_mfma_f32_16x16x32_bf16 v[116:119], v[72:75], v[172:175], v[116:119]\n"
      ".Lgq_pad2833:\n"
      "s_cbranch_execz .Lgq_pad2876\n"
      "ds_read_b64_tr_b16 v[72:73], v232 offset:46144\n"
      "ds_read_b64_tr_b16 v[74:75], v232 offset:50752\n"
      "v_mfma_f32_16x16x32_bf16 v[120:123], v[76:79], v[172:175], v[120:123]\n"
      "ds_read_b64_tr_b16 v[76:77], v232 offset:46176\n"
      "ds_read_b64_tr_b16 v[78:79], v232 offset:50784\n"
      "v_mfma_f32_16x16x32_bf16 v[124:127], v[80:83], v[172:175], v[124:127]\n"
      "ds_read_b64_tr_b16 v[80:81], v232 offset:46208\n"
      "ds_read_b64_tr_b16 v[82:83], v232 offset:50816\n"
      "v_mfma_f32_16x16x32_bf16 v[128:131], v[84:87], v[172:175], v[128:131]\n"
      "ds_read_b64_tr_b16 v[84:85], v232 offset:46240\n"
      "ds_read_b64_tr_b16 v[86:87], v232 offset:50848\n"
      "ds_read_b64_tr_b16 v[172:173], v232 offset:46272\n"
      "ds_read_b64_tr_b16 v[174:175], v232 offset:50880\n"
      "v_mfma_f32_16x16x32_bf16 v[96:99], v[88:91], v[176:179], v[96:99]\n"
      "ds_read_b64_tr_b16 v[88:89], v232 offset:46304\n"
      "ds_read_b64_tr_b16 v[90:91], v232 offset:50912\n"
      "v_mfma_f32_16x16x32_bf16 v[100:103], v[92:95], v[176:179], v[100:103]\n"
      "ds_read_b64_tr_b16 v[92:93], v232 offset:46336\n"
      "ds_read_b64_tr_b16 v[94:95], v232 offset:50944\n"
      "v_mfma_f32_16x16x32_bf16 v[104:107], v[132:135], v[176:179], v[104:107]\n"
      "s_bitcmp1_b64 s[36:37], 35\n"
      "s_cbranch_scc0 .Lgq_w41\n"
      ".Lgq_r41:\n"
      "ds_read_b64_tr_b16 v[132:133], v232 offset:55296\n"
      "ds_read_b64_tr_b16 v[134:135], v232 offset:59904\n"
      "v_mfma_f32_16x16x32_bf16 v[108:111], v[136:139], v[176:179], v[108:111]\n"
      "ds_read_b64_tr_b16 v[136:137], v232 offset:55328\n"
      "ds_read_b64_tr_b16 v[138:139], v232 offset:59936\n"
      "v_mfma_f32_16x16x32_bf16 v[112:115], v[140:143], v[176:179], v[112:115]\n"
      "ds_read_b64_tr_b16 v[140:141], v232 offset:55360\n"
      "ds_read_b64_tr_b16 v[142:143], v232 offset:59968\n"
      "v_mfma_f32_16x16x32_bf16 v[116:119], v[144:147], v[176:179], v[116:119]\n"
      "ds_read_b64_tr_b16 v[144:145], v232 offset:55392\n"
      "ds_read_b64_tr_b16 v[146:147], v232 offset:60000\n"
      "s_waitcnt vmcnt(0)\n"
      "buffer_load_dwordx4 v[184:187], v4, s[28:31], s45 offen offset:0 sc1\n"
      "buffer_load_dwordx4 v[188:191], v4, s[28:31], s45 offen offset:1024 sc1\n"
      "buffer_load_dwordx4 v[192:195], v4, s[28:31], s45 offen offset:2048 sc1\n"
      "buffer_load_dwordx4 v[196:199], v4, s[28:31], s45 offen offset:3072 sc1\n"
      "buffer_load_dwordx2 v[236:237], v5, s[28:31], s45 offen sc1\n"
      "v_mfma_f32_16x16x32_bf16 v[120:123], v[148:151], v[176:179], v[120:123]\n"
      ".Lgq_pad2876:\n"
      "s_cbranch_execz .Lgq_pad2913\n"
      "ds_read_b64_tr_b16 v[148:149], v232 offset:55424\n"
      "ds_read_b64_tr_b16 v[150:151], v232 offset:60032\n"
      "s_waitcnt lgkmcnt(14)\n"
      "v_mfma_f32_16x16x32_bf16 v[124:127], v[152:155], v[176:179], v[124:127]\n"
      "ds_read_b64_tr_b16 v[152:153], v232 offset:55456\n"
      "ds_read_b64_tr_b16 v[154:155], v232 offset:60064\n"
      "v_mfma_f32_16x16x32_bf16 v[128:131], v[156:159], v[176:179], v[128:131]\n"
      "ds_read_b64_tr_b16 v[156:157], v232 offset:55488\n"
      "ds_read_b64_tr_b16 v[158:159], v232 offset:60096\n"
      "ds_read_b64_tr_b16 v[176:177], v232 offset:55520\n"
      "ds_read_b64_tr_b16 v[178:179], v232 offset:60128\n"
      "v_mfma_f32_16x16x32_bf16 v[96:99], v[160:163], v[180:183], v[96:99]\n"
      "ds_read_b64_tr_b16 v[160:161], v232 offset:55552\n"
      "ds_read_b64_tr_b16 v[162:163], v232 offset:60160\n"
      "v_mfma_f32_16x16x32_bf16 v[100:103], v[164:167], v[180:183], v[100:103]\n"
      "ds_read_b64_tr_b16 v[164:165], v233 offset:0\n"
      "ds_read_b64_tr_b16 v[166:167], v233 offset:4608\n"
      "v_mfma_f32_16x16x32_bf16 v[104:107], v[244:247], v[180:183], v[104:107]\n"
      "ds_read_b64_tr_b16 v[244:245], v233 offset:32\n"
      "ds_read_b64_tr_b16 v[246:247], v233 offset:4640\n"
      "v_mfma_f32_16x16x32_bf16 v[108:111], v[248:251], v[180:183], v[108:111]\n"
      "ds_read_b64_tr_b16 v[248:249], v233 offset:64\n"
      "ds_read_b64_tr_b16 v[250:251], v233 offset:4672\n"
      "v_mfma_f32_16x16x32_bf16 v[112:115], v[252:255], v[180:183], v[112:115]\n"
      "ds_read_b64_tr_b16 v[252:253], v233 offset:96\n"
      "ds_read_b64_tr_b16 v[254:255], v233 offset:4704\n"
      "v_mfma_f32_16x16x32_bf16 v[116:119], v[16:19], v[180:183], v[116:119]\n"
      "ds_read_b64_tr_b16 v[16:17], v233 offset:128\n"
      "ds_read_b64_tr_b16 v[18:19], v233 offset:4736\n"
      "v_mfma_f32_16x16x32_bf16 v[120:123], v[20:23], v[180:183], v[120:123]\n"
      "ds_read_b64_tr_b16 v[20:21], v233 offset:160\n"
      "ds_read_b64_tr_b16 v[22:23], v233 offset:4768\n"
      "v_mfma_f32_16x16x32_bf16 v[124:127], v[24:27], v[180:183], v[124:127]\n"
      "ds_read_b64_tr_b16 v[24:25], v233 offset:192\n"
      "ds_read_b64_tr_b16 v[26:27], v233 offset:4800\n"
      "v_mfma_f32_16x16x32_bf16 v[128:131], v[28:31], v[180:183], v[128:131]\n"
      ".Lgq_pad2913:\n"
      "s_cbranch_execz .Lgq_pad2966\n"
      "ds_read_b64_tr_b16 v[28:29], v233 offset:224\n"
      "ds_read_b64_tr_b16 v[30:31], v233 offset:4832\n"
      "ds_read_b64_tr_b16 v[180:181], v233 offset:256\n"
      "ds_read_b64_tr_b16 v[182:183], v233 offset:4864\n"
      "s_waitcnt vmcnt(4)\n"
      "v_add3_u32 v6, s24, v184, v186\n"
      "v_add3_u32 v7, s25, v185, v187\n"
      "v_subrev_u32 v184, s24, v184\n"
      "s_waitcnt vmcnt(3)\n"
      "v_add3_u32 v6, v6, v188, v190\n"
      "v_add3_u32 v7, v7, v189, v191\n"
      "v_subrev_u32 v188, s24, v188\n"
      "s_waitcnt vmcnt(2)\n"
      "v_add3_u32 v6, v6, v192, v194\n"
      "v_add3_u32 v7, v7, v193, v195\n"
      "v_subrev_u32 v192, s24, v192\n"
      "s_waitcnt vmcnt(1)\n"
      "v_add3_u32 v6, v6, v196, v198\n"
      "v_add3_u32 v7, v7, v197, v199\n"
      "v_subrev_u32 v196, s24, v196\n"
      "s_waitcnt vmcnt(0)\n"
      "v_cmp_ne_u64 vcc, v[6:7], v[236:237]\n"
      "s_cbranch_vccnz .Lgq_reh1\n"
      ".Lgq_okh1:\n"
      "v_mfma_f32_16x16x32_bf16 v[96:99], v[32:35], v[184:187], v[96:99]\n"
      "buffer_load_dwordx4 v[200:203], v4, s[28:31], s46 offen offset:0 sc1\n"
      "s_bitcmp1_b64 s[36:37], 37\n"
      "s_cbranch_scc0 .Lgq_w42\n"
      ".Lgq_r42:\n"
      "ds_read_b64_tr_b16 v[32:33], v233 offset:9216\n"
      "ds_read_b64_tr_b16 v[34:35], v233 offset:13824\n"
      "v_mfma_f32_16x16x32_bf16 v[100:103], v[36:39], v[184:187], v[100:103]\n"
      "buffer_load_dwordx4 v[204:207], v4, s[28:31], s46 offen offset:1024 sc1\n"
      "ds_read_b64_tr_b16 v[36:37], v233 offset:9248\n"
      "v_mfma_f32_16x16x32_bf16 v[104:107], v[40:43], v[184:187], v[104:107]\n"
      "buffer_load_dwordx4 v[208:211], v4, s[28:31], s46 offen offset:2048 sc1\n"
      "ds_read_b64_tr_b16 v[38:39], v233 offset:13856\n"
      "ds_read_b64_tr_b16 v[40:41], v233 offset:9280\n"
      "v_mfma_f32_16x16x32_bf16 v[108:111], v[44:47], v[184:187], v[108:111]\n"
      "buffer_load_dwordx4 v[212:215], v4, s[28:31], s46 offen offset:3072 sc1\n"
      "ds_read_b64_tr_b16 v[42:43], v233 offset:13888\n"
      "v_mfma_f32_16x16x32_bf16 v[112:115], v[48:51], v[184:187], v[112:115]\n"
      "buffer_load_dwordx2 v[238:239], v5, s[28:31], s46 offen sc1\n"
      "ds_read_b64_tr_b16 v[44:45], v233 offset:9312\n"
      "ds_read_b64_tr_b16 v[46:47], v233 offset:13920\n"
      "v_mfma_f32_16x16x32_bf16 v[116:119], v[168:171], v[184:187], v[116:119]\n"
      "buffer_load_dwordx4 v[216:219], v4, s[28:31], s47 offen offset:0 sc1\n"
      "ds_read_b64_tr_b16 v[48:49], v233 offset:9344\n"
      "v_mfma_f32_16x16x32_bf16 v[120:123], v[52:55], v[184:187], v[120:123]\n"
      "buffer_load_dwordx4 v[220:223], v4, s[28:31], s47 offen offset:1024 sc1\n"
      "ds_read_b64_tr_b16 v[50:51], v233 offset:13952\n"
      "v_mfma_f32_16x16x32_bf16 v[124:127], v[56:59], v[184:187], v[124:127]\n"
      ".Lgq_pad2966:\n"
      "s_cbranch_execz .Lgq_pad3008\n"
      "buffer_load_dwordx4 v[224:227], v4, s[28:31], s47 offen offset:2048 sc1\n"
      "ds_read_b64_tr_b16 v[168:169], v233 offset:9376\n"
      "ds_read_b64_tr_b16 v[170:171], v233 offset:13984\n"
      "v_mfma_f32_16x16x32_bf16 v[128:131], v[60:63], v[184:187], v[128:131]\n"
      "buffer_load_dwordx4 v[228:231], v4, s[28:31], s47 offen offset:3072 sc1\n"
      "ds_read_b64_tr_b16 v[52:53], v233 offset:9408\n"
      "v_mfma_f32_16x16x32_bf16 v[96:99], v[64:67], v[188:191], v[96:99]\n"
      "buffer_load_dwordx2 v[240:241], v5, s[28:31], s47 offen sc1\n"
      "ds_read_b64_tr_b16 v[54:55], v233 offset:14016\n"
      "ds_read_b64_tr_b16 v[56:57], v233 offset:9440\n"
      "v_mfma_f32_16x16x32_bf16 v[100:103], v[68:71], v[188:191], v[100:103]\n"
      "ds_read_b64_tr_b16 v[58:59], v233 offset:14048\n"
      "v_mfma_f32_16x16x32_bf16 v[104:107], v[72:75], v[188:191], v[104:107]\n"
      "ds_read_b64_tr_b16 v[60:61], v233 offset:9472\n"
      "ds_read_b64_tr_b16 v[62:63], v233 offset:14080\n"
      "v_mfma_f32_16x16x32_bf16 v[108:111], v[76:79], v[188:191], v[108:111]\n"
      "ds_read_b64_tr_b16 v[64:65], v233 offset:18432\n"
      "v_mfma_f32_16x16x32_bf16 v[112:115], v[80:83], v[188:191], v[112:115]\n"
      "ds_read_b64_tr_b16 v[66:67], v233 offset:23040\n"
      "v_mfma_f32_16x16x32_bf16 v[116:119], v[84:87], v[188:191], v[116:119]\n"
      "ds_read_b64_tr_b16 v[68:69], v233 offset:18464\n"
      "ds_read_b64_tr_b16 v[70:71], v233 offset:23072\n"
      "v_mfma_f32_16x16x32_bf16 v[120:123], v[172:175], v[188:191], v[120:123]\n"
      "ds_read_b64_tr_b16 v[72:73], v233 offset:18496\n"
      "s_waitcnt lgkmcnt(14)\n"
      "v_mfma_f32_16x16x32_bf16 v[124:127], v[88:91], v[188:191], v[124:127]\n"
      "ds_read_b64_tr_b16 v[74:75], v233 offset:23104\n"
      "ds_read_b64_tr_b16 v[76:77], v233 offset:18528\n"
      "v_mfma_f32_16x16x32_bf16 v[128:131], v[92:95], v[188:191], v[128:131]\n"
      "ds_read_b64_tr_b16 v[78:79], v233 offset:23136\n"
      "v_mfma_f32_16x16x32_bf16 v[96:99], v[132:135], v[192:195], v[96:99]\n"
      "ds_read_b64_tr_b16 v[80:81], v233 offset:18560\n"
      "ds_read_b64_tr_b16 v[82:83], v233 offset:23168\n"
      "v_mfma_f32_16x16x32_bf16 v[100:103], v[136:139], v[192:195], v[100:103]\n"
      "ds_read_b64_tr_b16 v[84:85], v233 offset:18592\n"
      "v_mfma_f32_16x16x32_bf16 v[104:107], v[140:143], v[192:195], v[104:107]\n"
      "ds_read_b64_tr_b16 v[86:87], v233 offset:23200\n"
      "v_mfma_f32_16x16x32_bf16 v[108:111], v[144:147], v[192:195], v[108:111]\n"
      "ds_read_b64_tr_b16 v[172:173], v233 offset:18624\n"
      "ds_read_b64_tr_b16 v[174:175], v233 offset:23232\n"
      "v_mfma_f32_16x16x32_bf16 v[112:115], v[148:151], v[192:195], v[112:115]\n"
      ".Lgq_pad3008:\n"
      "s_cbranch_execz .Lgq_pad3046\n"
      "ds_read_b64_tr_b16 v[88:89], v233 offset:18656\n"
      "v_mfma_f32_16x16x32_bf16 v[116:119], v[152:155], v[192:195], v[116:119]\n"
      "ds_read_b64_tr_b16 v[90:91], v233 offset:23264\n"
      "ds_read_b64_tr_b16 v[92:93], v233 offset:18688\n"
      "v_mfma_f32_16x16x32_bf16 v[120:123], v[156:159], v[192:195], v[120:123]\n"
      "ds_read_b64_tr_b16 v[94:95], v233 offset:23296\n"
      "v_mfma_f32_16x16x32_bf16 v[124:127], v[176:179], v[192:195], v[124:127]\n"
      "s_bitcmp1_b64 s[36:37], 39\n"
      "s_cbranch_scc0 .Lgq_w43\n"
      ".Lgq_r43:\n"
      "ds_read_b64_tr_b16 v[132:133], v233 offset:27648\n"
      "ds_read_b64_tr_b16 v[134:135], v233 offset:32256\n"
      "v_mfma_f32_16x16x32_bf16 v[128:131], v[160:163], v[192:195], v[128:131]\n"
      "ds_read_b64_tr_b16 v[136:137], v233 offset:27680\n"
      "v_mfma_f32_16x16x32_bf16 v[96:99], v[164:167], v[196:199], v[96:99]\n"
      "ds_read_b64_tr_b16 v[138:139], v233 offset:32288\n"
      "v_mfma_f32_16x16x32_bf16 v[100:103], v[244:247], v[196:199], v[100:103]\n"
      "ds_read_b64_tr_b16 v[140:141], v233 offset:36864\n"
      "ds_read_b64_tr_b16 v[142:143], v233 offset:41472\n"
      "v_mfma_f32_16x16x32_bf16 v[104:107], v[248:251], v[196:199], v[104:107]\n"
      "ds_read_b64_tr_b16 v[144:145], v233 offset:36896\n"
      "v_mfma_f32_16x16x32_bf16 v[108:111], v[252:255], v[196:199], v[108:111]\n"
      "ds_read_b64_tr_b16 v[146:147], v233 offset:41504\n"
      "s_bitcmp1_b64 s[36:37], 41\n"
      "s_cbranch_scc0 .Lgq_w44\n"
      ".Lgq_r44:\n"
      "ds_read_b64_tr_b16 v[148:149], v233 offset:46080\n"
      "v_mfma_f32_16x16x32_bf16 v[112:115], v[16:19], v[196:199], v[112:115]\n"
      "ds_read_b64_tr_b16 v[150:151], v233 offset:50688\n"
      "v_mfma_f32_16x16x32_bf16 v[116:119], v[20:23], v[196:199], v[116:119]\n"
      "ds_read_b64_tr_b16 v[152:153], v233 offset:46112\n"
      "ds_read_b64_tr_b16 v[154:155], v233 offset:50720\n"
      "v_mfma_f32_16x16x32_bf16 v[120:123], v[24:27], v[196:199], v[120:123]\n"
      "ds_read_b64_tr_b16 v[156:157], v233 offset:55296\n"
      "v_mfma_f32_16x16x32_bf16 v[124:127], v[28:31], v[196:199], v[124:127]\n"
      "ds_read_b64_tr_b16 v[158:159], v233 offset:59904\n"
      "v_mfma_f32_16x16x32_bf16 v[128:131], v[180:183], v[196:199], v[128:131]\n"
      ".Lgq_pad3046:\n"
      "s_cbranch_execz .Lgq_pad3101\n"
      "ds_read_b64_tr_b16 v[176:177], v233 offset:55328\n"
      "ds_read_b64_tr_b16 v[178:179], v233 offset:59936\n"
      "s_waitcnt vmcnt(9)\n"
      "v_add3_u32 v6, s24, v200, v202\n"
      "v_add3_u32 v7, s25, v201, v203\n"
      "v_subrev_u32 v200, s24, v200\n"
      "s_waitcnt vmcnt(8)\n"
      "v_add3_u32 v6, v6, v204, v206\n"
      "v_add3_u32 v7, v7, v205, v207\n"
      "v_subrev_u32 v204, s24, v204\n"
      "s_waitcnt vmcnt(7)\n"
      "v_add3_u32 v6, v6, v208, v210\n"
      "v_add3_u32 v7, v7, v209, v211\n"
      "v_subrev_u32 v208, s24, v208\n"
      "s_waitcnt vmcnt(6)\n"
      "v_add3_u32 v6, v6, v212, v214\n"
      "v_add3_u32 v7, v7, v213, v215\n"
      "v_subrev_u32 v212, s24, v212\n"
      "s_waitcnt vmcnt(5)\n"
      "v_cmp_ne_u64 vcc, v[6:7], v[238:239]\n"
      "s_cbranch_vccnz .Lgq_reh2\n"
      ".Lgq_okh2:\n"
      "v_mfma_f32_16x16x32_bf16 v[96:99], v[32:35], v[200:203], v[96:99]\n"
      "s_bitcmp1_b64 s[36:37], 43\n"
      "s_cbranch_scc0 .Lgq_w45\n"
      ".Lgq_r45:\n"
      "ds_read_b64_tr_b16 v[160:161], v234 offset:0\n"
      "v_mfma_f32_16x16x32_bf16 v[100:103], v[36:39], v[200:203], v[100:103]\n"
      "ds_read_b64_tr_b16 v[162:163], v234 offset:4608\n"
      "ds_read_b64_tr_b16 v[164:165], v234 offset:32\n"
      "v_mfma_f32_16x16x32_bf16 v[104:107], v[40:43], v[200:203], v[104:107]\n"
      "ds_read_b64_tr_b16 v[166:167], v234 offset:4640\n"
      "v_mfma_f32_16x16x32_bf16 v[108:111], v[44:47], v[200:203], v[108:111]\n"
      "ds_read_b64_tr_b16 v[244:245], v234 offset:9216\n"
      "ds_read_b64_tr_b16 v[246:247], v234 offset:13824\n"
      "s_waitcnt lgkmcnt(14)\n"
      "v_mfma_f32_16x16x32_bf16 v[112:115], v[48:51], v[200:203], v[112:115]\n"
      "ds_read_b64_tr_b16 v[248:249], v234 offset:9248\n"
      "v_mfma_f32_16x16x32_bf16 v[116:119], v[168:171], v[200:203], v[116:119]\n"
      "ds_read_b64_tr_b16 v[250:251], v234 offset:13856\n"
      "v_mfma_f32_16x16x32_bf16 v[120:123], v[52:55], v[200:203], v[120:123]\n"
      "ds_read_b64_tr_b16 v[252:253], v233 offset:27712\n"
      "ds_read_b64_tr_b16 v[254:255], v233 offset:32320\n"
      "v_mfma_f32_16x16x32_bf16 v[124:127], v[56:59], v[200:203], v[124:127]\n"
      "ds_read_b64_tr_b16 v[16:17], v233 offset:27744\n"
      "s_waitcnt vmcnt(0)\n"
      "v_mfma_f32_16x16x32_bf16 v[128:131], v[60:63], v[200:203], v[128:131]\n"
      "ds_read_b64_tr_b16 v[18:19], v233 offset:32352\n"
      "ds_read_b64_tr_b16 v[20:21], v233 offset:36928\n"
      "v_add3_u32 v6, s24, v216, v218\n"
      "v_mfma_f32_16x16x32_bf16 v[96:99], v[64:67], v[204:207], v[96:99]\n"
      "ds_read_b64_tr_b16 v[22:23], v233 offset:41536\n"
      "v_add3_u32 v7, s25, v217, v219\n"
      "v_mfma_f32_16x16x32_bf16 v[100:103], v[68:71], v[204:207], v[100:103]\n"
      ".Lgq_pad3101:\n"
      "s_cbranch_execz .Lgq_pad3136\n"
      "ds_read_b64_tr_b16 v[24:25], v233 offset:36960\n"
      "ds_read_b64_tr_b16 v[26:27], v233 offset:41568\n"
      "v_subrev_u32 v216, s24, v216\n"
      "v_mfma_f32_16x16x32_bf16 v[104:107], v[72:75], v[204:207], v[104:107]\n"
      "ds_read_b64_tr_b16 v[28:29], v233 offset:46144\n"
      "v_add3_u32 v6, v6, v220, v222\n"
      "v_mfma_f32_16x16x32_bf16 v[108:111], v[76:79], v[204:207], v[108:111]\n"
      "ds_read_b64_tr_b16 v[30:31], v233 offset:50752\n"
      "v_add3_u32 v7, v7, v221, v223\n"
      "v_mfma_f32_16x16x32_bf16 v[112:115], v[80:83], v[204:207], v[112:115]\n"
      "ds_read_b64_tr_b16 v[180:181], v233 offset:46176\n"
      "ds_read_b64_tr_b16 v[182:183], v233 offset:50784\n"
      "v_subrev_u32 v220, s24, v220\n"
      "v_mfma_f32_16x16x32_bf16 v[116:119], v[84:87], v[204:207], v[116:119]\n"
      "ds_read_b64_tr_b16 v[32:33], v233 offset:55360\n"
      "v_add3_u32 v6, v6, v224, v226\n"
      "v_mfma_f32_16x16x32_bf16 v[120:123], v[172:175], v[204:207], v[120:123]\n"
      "ds_read_b64_tr_b16 v[34:35], v233 offset:59968\n"
      "ds_read_b64_tr_b16 v[36:37], v233 offset:55392\n"
      "v_add3_u32 v7, v7, v225, v227\n"
      "v_mfma_f32_16x16x32_bf16 v[124:127], v[88:91], v[204:207], v[124:127]\n"
      "ds_read_b64_tr_b16 v[38:39], v233 offset:60000\n"
      "v_subrev_u32 v224, s24, v224\n"
      "v_mfma_f32_16x16x32_bf16 v[128:131], v[92:95], v[204:207], v[128:131]\n"
      "ds_read_b64_tr_b16 v[40:41], v234 offset:64\n"
      "v_add3_u32 v6, v6, v228, v230\n"
      "v_mfma_f32_16x16x32_bf16 v[96:99], v[132:135], v[208:211], v[96:99]\n"
      "ds_read_b64_tr_b16 v[42:43], v234 offset:4672\n"
      "ds_read_b64_tr_b16 v[44:45], v234 offset:96\n"
      "v_add3_u32 v7, v7, v229, v231\n"
      "v_mfma_f32_16x16x32_bf16 v[100:103], v[136:139], v[208:211], v[100:103]\n"
      "ds_read_b64_tr_b16 v[46:47], v234 offset:4704\n"
      "v_subrev_u32 v228, s24, v228\n"
      "v_mfma_f32_16x16x32_bf16 v[96:99], v[140:143], v[212:215], v[96:99]\n"
      ".Lgq_pad3136:\n"
      "s_cbranch_execz .Lgq_pad3177\n"
      "ds_read_b64_tr_b16 v[48:49], v234 offset:9280\n"
      "ds_read_b64_tr_b16 v[50:51], v234 offset:13888\n"
      "v_cmp_ne_u64 vcc, v[6:7], v[240:241]\n"
      "v_mfma_f32_16x16x32_bf16 v[100:103], v[144:147], v[212:215], v[100:103]\n"
      "ds_read_b64_tr_b16 v[168:169], v234 offset:9312\n"
      "s_cbranch_vccnz .Lgq_reh3\n"
      ".Lgq_okh3:\n"
      "s_waitcnt lgkmcnt(14)\n"
      "v_mfma_f32_16x16x32_bf16 v[96:99], v[148:151], v[216:219], v[96:99]\n"
      "ds_read_b64_tr_b16 v[170:171], v234 offset:13920\n"
      "ds_read_b64_tr_b16 v[52:53], v233 offset:27776\n"
      "v_mfma_f32_16x16x32_bf16 v[100:103], v[152:155], v[216:219], v[100:103]\n"
      "ds_read_b64_tr_b16 v[54:55], v233 offset:32384\n"
      "v_mfma_f32_16x16x32_bf16 v[96:99], v[156:159], v[220:223], v[96:99]\n"
      "ds_read_b64_tr_b16 v[56:57], v233 offset:27808\n"
      "v_mfma_f32_16x16x32_bf16 v[100:103], v[176:179], v[220:223], v[100:103]\n"
      "ds_read_b64_tr_b16 v[58:59], v233 offset:32416\n"
      "ds_read_b64_tr_b16 v[60:61], v233 offset:36992\n"
      "v_mfma_f32_16x16x32_bf16 v[96:99], v[160:163], v[224:227], v[96:99]\n"
      "ds_read_b64_tr_b16 v[62:63], v233 offset:41600\n"
      "v_mfma_f32_16x16x32_bf16 v[100:103], v[164:167], v[224:227], v[100:103]\n"
      "ds_read_b64_tr_b16 v[64:65], v233 offset:37024\n"
      "ds_read_b64_tr_b16 v[66:67], v233 offset:41632\n"
      "v_mfma_f32_16x16x32_bf16 v[96:99], v[244:247], v[228:231], v[96:99]\n"
      "ds_read_b64_tr_b16 v[68:69], v233 offset:46208\n"
      "v_mfma_f32_16x16x32_bf16 v[100:103], v[248:251], v[228:231], v[100:103]\n"
      "ds_read_b64_tr_b16 v[70:71], v233 offset:50816\n"
      "ds_read_b64_tr_b16 v[72:73], v233 offset:46240\n"
      "v_mfma_f32_16x16x32_bf16 v[104:107], v[252:255], v[208:211], v[104:107]\n"
      "ds_read_b64_tr_b16 v[74:75], v233 offset:50848\n"
      "v_mfma_f32_16x16x32_bf16 v[108:111], v[16:19], v[208:211], v[108:111]\n"
      "ds_read_b64_tr_b16 v[76:77], v233 offset:55424\n"
      "v_mfma_f32_16x16x32_bf16 v[104:107], v[20:23], v[212:215], v[104:107]\n"
      "ds_read_b64_tr_b16 v[78:79], v233 offset:60032\n"
      "ds_read_b64_tr_b16 v[80:81], v233 offset:55456\n"
      "v_mfma_f32_16x16x32_bf16 v[108:111], v[24:27], v[212:215], v[108:111]\n"
      "ds_read_b64_tr_b16 v[82:83], v233 offset:60064\n"
      "v_cvt_pk_bf16_f32 v184, v96, v97\n"
      "s_waitcnt lgkmcnt(14)\n"
      "v_mfma_f32_16x16x32_bf16 v[104:107], v[28:31], v[216:219], v[104:107]\n"
      ".Lgq_pad3177:\n"
      "s_cbranch_execz .Lgq_pad3220\n"
      "ds_read_b64_tr_b16 v[84:85], v234 offset:128\n"
      "ds_read_b64_tr_b16 v[86:87], v234 offset:4736\n"
      "v_cvt_pk_bf16_f32 v185, v98, v99\n"
      "v_mfma_f32_16x16x32_bf16 v[108:111], v[180:183], v[216:219], v[108:111]\n"
      "ds_read_b64_tr_b16 v[172:173], v234 offset:160\n"
      "v_add_u32 v184, s24, v184\n"
      "v_mfma_f32_16x16x32_bf16 v[104:107], v[32:35], v[220:223], v[104:107]\n"
      "ds_read_b64_tr_b16 v[174:175], v234 offset:4768\n"
      "ds_read_b64_tr_b16 v[88:89], v234 offset:9344\n"
      "v_cvt_pk_bf16_f32 v186, v100, v101\n"
      "v_mfma_f32_16x16x32_bf16 v[108:111], v[36:39], v[220:223], v[108:111]\n"
      "ds_read_b64_tr_b16 v[90:91], v234 offset:13952\n"
      "v_cvt_pk_bf16_f32 v187, v102, v103\n"
      "v_mfma_f32_16x16x32_bf16 v[104:107], v[40:43], v[224:227], v[104:107]\n"
      "ds_read_b64_tr_b16 v[92:93], v234 offset:9376\n"
      "buffer_store_dwordx4 v[184:187], v13, s[28:31], s48 offen offset:0 sc1\n"
      "v_mfma_f32_16x16x32_bf16 v[108:111], v[44:47], v[224:227], v[108:111]\n"
      "ds_read_b64_tr_b16 v[94:95], v234 offset:13984\n"
      "ds_read_b64_tr_b16 v[132:133], v233 offset:27840\n"
      "v_add3_u32 v202, s24, v184, v186\n"
      "v_mfma_f32_16x16x32_bf16 v[104:107], v[48:51], v[228:231], v[104:107]\n"
      "ds_read_b64_tr_b16 v[134:135], v233 offset:32448\n"
      "v_add3_u32 v203, s25, v185, v187\n"
      "v_mfma_f32_16x16x32_bf16 v[108:111], v[168:171], v[228:231], v[108:111]\n"
      "ds_read_b64_tr_b16 v[136:137], v233 offset:27872\n"
      "ds_read_b64_tr_b16 v[138:139], v233 offset:32480\n"
      "v_mfma_f32_16x16x32_bf16 v[112:115], v[52:55], v[208:211], v[112:115]\n"
      "ds_read_b64_tr_b16 v[140:141], v233 offset:37056\n"
      "s_waitcnt lgkmcnt(14)\n"
      "v_mfma_f32_16x16x32_bf16 v[116:119], v[56:59], v[208:211], v[116:119]\n"
      "ds_read_b64_tr_b16 v[142:143], v233 offset:41664\n"
      "ds_read_b64_tr_b16 v[144:145], v233 offset:37088\n"
      "v_mfma_f32_16x16x32_bf16 v[112:115], v[60:63], v[212:215], v[112:115]\n"
      "ds_read_b64_tr_b16 v[146:147], v233 offset:41696\n"
      "v_mfma_f32_16x16x32_bf16 v[116:119], v[64:67], v[212:215], v[116:119]\n"
      "ds_read_b64_tr_b16 v[148:149], v233 offset:46272\n"
      "v_cvt_pk_bf16_f32 v188, v104, v105\n"
      "v_mfma_f32_16x16x32_bf16 v[112:115], v[68:71], v[216:219], v[112:115]\n"
      "ds_read_b64_tr_b16 v[150:151], v233 offset:50880\n"
      "ds_read_b64_tr_b16 v[152:153], v233 offset:46304\n"
      "v_cvt_pk_bf16_f32 v189, v106, v107\n"
      "v_mfma_f32_16x16x32_bf16 v[116:119], v[72:75], v[216:219], v[116:119]\n"
      ".Lgq_pad3220:\n"
      "s_cbranch_execz .Lgq_pad3264\n"
      "ds_read_b64_tr_b16 v[154:155], v233 offset:50912\n"
      "v_add_u32 v188, s24, v188\n"
      "v_mfma_f32_16x16x32_bf16 v[112:115], v[76:79], v[220:223], v[112:115]\n"
      "ds_read_b64_tr_b16 v[156:157], v233 offset:55488\n"
      "ds_read_b64_tr_b16 v[158:159], v233 offset:60096\n"
      "v_cvt_pk_bf16_f32 v190, v108, v109\n"
      "s_waitcnt lgkmcnt(14)\n"
      "v_mfma_f32_16x16x32_bf16 v[116:119], v[80:83], v[220:223], v[116:119]\n"
      "ds_read_b64_tr_b16 v[176:177], v233 offset:55520\n"
      "v_cvt_pk_bf16_f32 v191, v110, v111\n"
      "v_mfma_f32_16x16x32_bf16 v[112:115], v[84:87], v[224:227], v[112:115]\n"
      "ds_read_b64_tr_b16 v[178:179], v233 offset:60128\n"
      "ds_read_b64_tr_b16 v[160:161], v234 offset:192\n"
      "buffer_store_dwordx4 v[188:191], v13, s[28:31], s48 offen offset:512 sc1\n"
      "v_mfma_f32_16x16x32_bf16 v[116:119], v[172:175], v[224:227], v[116:119]\n"
      "ds_read_b64_tr_b16 v[162:163], v234 offset:4800\n"
      "v_add3_u32 v202, v202, v188, v190\n"
      "v_mfma_f32_16x16x32_bf16 v[112:115], v[88:91], v[228:231], v[112:115]\n"
      "ds_read_b64_tr_b16 v[164:165], v234 offset:224\n"
      "v_add3_u32 v203, v203, v189, v191\n"
      "v_mfma_f32_16x16x32_bf16 v[116:119], v[92:95], v[228:231], v[116:119]\n"
      "ds_read_b64_tr_b16 v[166:167], v234 offset:4832\n"
      "ds_read_b64_tr_b16 v[244:245], v234 offset:9408\n"
      "s_waitcnt lgkmcnt(11)\n"
      "v_mfma_f32_16x16x32_bf16 v[120:123], v[132:135], v[208:211], v[120:123]\n"
      "ds_read_b64_tr_b16 v[246:247], v234 offset:14016\n"
      "v_mfma_f32_16x16x32_bf16 v[124:127], v[136:139], v[208:211], v[124:127]\n"
      "ds_read_b64_tr_b16 v[248:249], v234 offset:9440\n"
      "ds_read_b64_tr_b16 v[250:251], v234 offset:14048\n"
      "v_mfma_f32_16x16x32_bf16 v[120:123], v[140:143], v[212:215], v[120:123]\n"
      "ds_read_b64_tr_b16 v[252:253], v233 offset:27904\n"
      "v_mfma_f32_16x16x32_bf16 v[124:127], v[144:147], v[212:215], v[124:127]\n"
      "ds_read_b64_tr_b16 v[254:255], v233 offset:32512\n"
      "ds_read_b64_tr_b16 v[16:17], v233 offset:37120\n"
      "v_cvt_pk_bf16_f32 v192, v112, v113\n"
      "v_mfma_f32_16x16x32_bf16 v[120:123], v[148:151], v[216:219], v[120:123]\n"
      "ds_read_b64_tr_b16 v[18:19], v233 offset:41728\n"
      "v_cvt_pk_bf16_f32 v193, v114, v115\n"
      "s_waitcnt lgkmcnt(8)\n"
      "v_mfma_f32_16x16x32_bf16 v[124:127], v[152:155], v[216:219], v[124:127]\n"
      "ds_read_b64_tr_b16 v[20:21], v233 offset:46336\n"
      "v_add_u32 v192, s24, v192\n"
      "v_mfma_f32_16x16x32_bf16 v[120:123], v[156:159], v[220:223], v[120:123]\n"
      ".Lgq_pad3264:\n"
      "s_cbranch_execz .Lgq_pad3294\n"
      "ds_read_b64_tr_b16 v[22:23], v233 offset:50944\n"
      "ds_read_b64_tr_b16 v[24:25], v233 offset:55552\n"
      "v_cvt_pk_bf16_f32 v194, v116, v117\n"
      "v_mfma_f32_16x16x32_bf16 v[124:127], v[176:179], v[220:223], v[124:127]\n"
      "ds_read_b64_tr_b16 v[26:27], v233 offset:60160\n"
      "v_cvt_pk_bf16_f32 v195, v118, v119\n"
      "v_mfma_f32_16x16x32_bf16 v[120:123], v[160:163], v[224:227], v[120:123]\n"
      "ds_read_b64_tr_b16 v[28:29], v234 offset:256\n"
      "ds_read_b64_tr_b16 v[30:31], v234 offset:4864\n"
      "buffer_store_dwordx4 v[192:195], v13, s[28:31], s48 offen offset:1024 sc1\n"
      "v_mfma_f32_16x16x32_bf16 v[124:127], v[164:167], v[224:227], v[124:127]\n"
      "ds_read_b64_tr_b16 v[180:181], v234 offset:9472\n"
      "v_add3_u32 v202, v202, v192, v194\n"
      "s_waitcnt lgkmcnt(5)\n"
      "v_mfma_f32_16x16x32_bf16 v[120:123], v[244:247], v[228:231], v[120:123]\n"
      "ds_read_b64_tr_b16 v[182:183], v234 offset:14080\n"
      "v_add3_u32 v203, v203, v193, v195\n"
      "v_mfma_f32_16x16x32_bf16 v[124:127], v[248:251], v[228:231], v[124:127]\n"
      "v_mfma_f32_16x16x32_bf16 v[128:131], v[252:255], v[208:211], v[128:131]\n"
      "v_mfma_f32_16x16x32_bf16 v[128:131], v[16:19], v[212:215], v[128:131]\n"
      "v_mfma_f32_16x16x32_bf16 v[128:131], v[20:23], v[216:219], v[128:131]\n"
      "s_waitcnt lgkmcnt(0)\n"
      "v_mfma_f32_16x16x32_bf16 v[128:131], v[24:27], v[220:223], v[128:131]\n"
      "v_cvt_pk_bf16_f32 v196, v120, v121\n"
      "v_cvt_pk_bf16_f32 v197, v122, v123\n"
      "v_mfma_f32_16x16x32_bf16 v[128:131], v[28:31], v[224:227], v[128:131]\n"
      "v_add_u32 v196, s24, v196\n"
      "v_cvt_pk_bf16_f32 v198, v124, v125\n"
      "v_mfma_f32_16x16x32_bf16 v[128:131], v[180:183], v[228:231], v[128:131]\n"
      ".Lgq_pad3294:\n"
      "s_cbranch_execz .Lgq_pad3358\n"
      "v_cvt_pk_bf16_f32 v199, v126, v127\n"
      "buffer_store_dwordx4 v[196:199], v13, s[28:31], s48 offen offset:1536 sc1\n"
      "v_add3_u32 v202, v202, v196, v198\n"
      "v_add3_u32 v203, v203, v197, v199\n"
      "s_nop 3\n"
      "v_cvt_pk_bf16_f32 v200, v128, v129\n"
      "v_cvt_pk_bf16_f32 v201, v130, v131\n"
      "v_add_u32 v200, s24, v200\n"
      "v_add_u32 v202, v202, v200\n"
      "v_add_u32 v203, v203, v201\n"
      "buffer_store_dwordx4 v[200:203], v13, s[28:31], s48 offen offset:2048 sc1\n"
      "v_and_b32 v14, 1, v1\n"
      "v_lshlrev_b32 v14, 4, v14\n"
      "v_mov_b32 v15, 0x3f80\n"
      "v_lshlrev_b32 v14, v14, v15\n"
      "v_lshrrev_b32 v15, 1, v2\n"
      "v_cmp_eq_u32 vcc, 0, v15\n"
      "s_nop 1\n"
      "v_cndmask_b32 v40, 0, v14, vcc\n"
      "v_cmp_eq_u32 vcc, 1, v15\n"
      "s_nop 1\n"
      "v_cndmask_b32 v41, 0, v14, vcc\n"
      "v_cmp_eq_u32 vcc, 2, v15\n"
      "s_nop 1\n"
      "v_cndmask_b32 v42, 0, v14, vcc\n"
      "v_cmp_eq_u32 vcc, 3, v15\n"
      "s_nop 1\n"
      "v_cndmask_b32 v43, 0, v14, vcc\n"
      "v_cmp_eq_u32 vcc, 4, v15\n"
      "s_nop 1\n"
      "v_cndmask_b32 v44, 0, v14, vcc\n"
      "v_cmp_eq_u32 vcc, 5, v15\n"
      "s_nop 1\n"
      "v_cndmask_b32 v45, 0, v14, vcc\n"
      "v_cmp_eq_u32 vcc, 6, v15\n"
      "s_nop 1\n"
      "v_cndmask_b32 v46, 0, v14, vcc\n"
      "v_cmp_eq_u32 vcc, 7, v15\n"
      "s_nop 1\n"
      "v_cndmask_b32 v47, 0, v14, vcc\n"
      "v_lshrrev_b32 v14, 3, v2\n"
      "v_and_b32 v15, 7, v2\n"
      "v_lshl_add_u32 v50, v14, 2, v3\n"
      "v_mul_u32_u24 v48, 2560, v50\n"
      "v_lshl_add_u32 v48, v15, 4, v48\n"
      "v_mul_u32_u24 v49, 1152, v15\n"
      "v_lshl_add_u32 v49, v50, 5, v49\n"
      "v_mov_b32 v38, 0\n"
      "v_mov_b32 v39, 0\n"
      "s_sleep 23\n"
      "buffer_load_dwordx4 v[16:19], v48, s[28:31], s49 offen offset:0 sc1\n"
      "buffer_load_dwordx4 v[20:23], v48, s[28:31], s49 offen offset:512 sc1\n"
      "buffer_load_dwordx4 v[24:27], v48, s[28:31], s49 offen offset:1024 sc1\n"
      "buffer_load_dwordx4 v[28:31], v48, s[28:31], s49 offen offset:1536 sc1\n"
      "buffer_load_dwordx4 v[32:35], v48, s[28:31], s49 offen offset:2048 sc1\n"
      ".Lgq_fin:\n"
      "s_waitcnt vmcnt(4)\n"
      "v_add3_u32 v6, s24, v16, v18\n"
      "v_subrev_u32 v16, s24, v16\n"
      "v_add3_u32 v7, s25, v17, v19\n"
      "s_nop 0\n"
      "v_mfma_f32_16x16x32_bf16 v[72:75], v[40:43], v[16:19], 0\n"
      ".Lgq_pad3358:\n"
      "s_cbranch_execz .Lgq_pad3416\n"
      "s_waitcnt vmcnt(3)\n"
      "v_add3_u32 v6, v6, v20, v22\n"
      "v_subrev_u32 v20, s24, v20\n"
      "v_add3_u32 v7, v7, v21, v23\n"
      "s_nop 0\n"
      "v_mfma_f32_16x16x32_bf16 v[72:75], v[44:47], v[20:23], v[72:75]\n"
      "s_waitcnt vmcnt(2)\n"
      "v_add3_u32 v6, v6, v24, v26\n"
      "v_subrev_u32 v24, s24, v24\n"
      "v_add3_u32 v7, v7, v25, v27\n"
      "s_nop 0\n"
      "v_mfma_f32_16x16x32_bf16 v[76:79], v[40:43], v[24:27], 0\n"
      "s_waitcnt vmcnt(1)\n"
      "v_add3_u32 v6, v6, v28, v30\n"
      "v_subrev_u32 v28, s24, v28\n"
      "v_add3_u32 v7, v7, v29, v31\n"
      "s_nop 0\n"
      "v_mfma_f32_16x16x32_bf16 v[76:79], v[44:47], v[28:31], v[76:79]\n"
      "s_waitcnt vmcnt(0)\n"
      "v_add_u32 v6, v6, v32\n"
      "v_subrev_u32 v32, s24, v32\n"
      "v_add_u32 v7, v7, v33\n"
      "v_mov_b32 v36, v32\n"
      "v_mov_b32 v37, v33\n"
      "v_cmp_ne_u64 vcc, v[6:7], v[34:35]\n"
      "s_nop 0\n"
      "v_mfma_f32_16x16x32_bf16 v[80:83], v[40:43], v[36:39], 0\n"
      "s_cbranch_vccnz .Lgq_rep\n"
      "v_add_f32_dpp v84, v72, v72 row_ror:8 row_mask:0xf bank_mask:0x3\n"
      "v_add_f32_dpp v85, v73, v73 row_ror:8 row_mask:0xf bank_mask:0x3\n"
      "v_add_f32_dpp v86, v74, v74 row_ror:8 row_mask:0xf bank_mask:0x3\n"
      "v_add_f32_dpp v87, v75, v75 row_ror:8 row_mask:0xf bank_mask:0x3\n"
      "v_add_f32_dpp v84, v76, v76 row_ror:8 row_mask:0xf bank_mask:0xc\n"
      "v_add_f32_dpp v85, v77, v77 row_ror:8 row_mask:0xf bank_mask:0xc\n"
      "v_add_f32_dpp v86, v78, v78 row_ror:8 row_mask:0xf bank_mask:0xc\n"
      "v_add_f32_dpp v87, v79, v79 row_ror:8 row_mask:0xf bank_mask:0xc\n"
      "v_cvt_pk_bf16_f32 v84, v84, v85\n"
      "v_cvt_pk_bf16_f32 v85, v86, v87\n"
      "buffer_store_dwordx2 v[84:85], v49, s[52:55], s50 offen\n"
      "v_add_f32_dpp v88, v80, v80 row_ror:8 row_mask:0xf bank_mask:0xf\n"
      "v_add_f32_dpp v89, v81, v81 row_ror:8 row_mask:0xf bank_mask:0xf\n"
      "v_add_f32_dpp v90, v82, v82 row_ror:8 row_mask:0xf bank_mask:0xf\n"
      "v_add_f32_dpp v91, v83, v83 row_ror:8 row_mask:0xf bank_mask:0xf\n"
      "v_cvt_pk_bf16_f32 v88, v88, v89\n"
      "v_cvt_pk_bf16_f32 v89, v90, v91\n"
      "s_mov_b64 exec, 0xff\n"
      "buffer_store_dwordx2 v[88:89], v49, s[52:55], s50 offen offset:256\n"
      "s_and_b32 s27, %1, 31\n"
      "s_or_b32 s27, s27, s33\n"
      "s_cbranch_scc1 .Lgq_end\n"
      "v_mov_b32 v8, s24\n"
      "v_mov_b32 v9, s25\n"
      "s_mov_b64 exec, 1\n"
      "buffer_store_dwordx2 v[8:9], off, s[28:31], 0 sc1\n"
      ".Lgq_end:\n"
      "s_endpgm\n"
      ".Lgq_pad3416:\n"
      "s_cbranch_execz .Lgq_walked\n"
      ".Lgq_rarep0:\n"
      "s_nop 7\n"
      "v_max3_f32 v204, v184, v185, v186\n"
      "v_max3_f32 v204, v204, v187, v188\n"
      "v_max3_f32 v204, v204, v189, v190\n"
      "v_max_f32 v204, v204, v191\n"
      "v_mov_b32 v205, v204\n"
      "s_nop 1\n"
      "v_permlane16_swap_b32 v205, v204\n"
      "v_max_f32 v204, v204, v205\n"
      "v_mov_b32 v205, v204\n"
      "s_nop 1\n"
      "v_permlane32_swap_b32 v205, v204\n"
      "v_max_f32 v204, v204, v205\n"
      "v_add_f32 v204, 0x41700000, v204\n"
      "v_max_f32 v204, 0, v204\n"
      "v_exp_f32 v205, -v204\n"
      "v_sub_f32 v24, v24, v204\n"
      "v_sub_f32 v25, v25, v204\n"
      "v_sub_f32 v26, v26, v204\n"
      "v_sub_f32 v27, v27, v204\n"
      "v_sub_f32 v184, v184, v204\n"
      "v_sub_f32 v96, v96, v204\n"
      "v_sub_f32 v185, v185, v204\n"
      "v_sub_f32 v97, v97, v204\n"
      "v_sub_f32 v186, v186, v204\n"
      "v_sub_f32 v98, v98, v204\n"
      "v_sub_f32 v187, v187, v204\n"
      "v_sub_f32 v99, v99, v204\n"
      "v_sub_f32 v188, v188, v204\n"
      "v_sub_f32 v100, v100, v204\n"
      "v_sub_f32 v189, v189, v204\n"
      "v_sub_f32 v101, v101, v204\n"
      "v_sub_f32 v190, v190, v204\n"
      "v_sub_f32 v102, v102, v204\n"
      "v_sub_f32 v191, v191, v204\n"
      "v_sub_f32 v103, v103, v204\n"
      "v_exp_f32 v192, v184\n"
      "v_exp_f32 v193, v185\n"
      "v_exp_f32 v194, v186\n"
      "v_exp_f32 v195, v187\n"
      "v_exp_f32 v196, v188\n"
      "v_exp_f32 v197, v189\n"
      "v_exp_f32 v198, v190\n"
      "v_exp_f32 v199, v191\n"
      "v_mul_f32 v132, v205, v132\n"
      "v_mul_f32 v133, v205, v133\n"
      "v_mul_f32 v134, v205, v134\n"
      "v_mul_f32 v135, v205, v135\n"
      "v_mul_f32 v136, v205, v136\n"
      "v_mul_f32 v137, v205, v137\n"
      "v_mul_f32 v138, v205, v138\n"
      "v_mul_f32 v139, v205, v139\n"
      "v_mul_f32 v140, v205, v140\n"
      "v_mul_f32 v141, v205, v141\n"
      "v_mul_f32 v142, v205, v142\n"
      "v_mul_f32 v143, v205, v143\n"
      "v_mul_f32 v144, v205, v144\n"
      "v_mul_f32 v145, v205, v145\n"
      "v_mul_f32 v146, v205, v146\n"
      "v_mul_f32 v147, v205, v147\n"
      "v_mul_f32 v148, v205, v148\n"
      "v_mul_f32 v149, v205, v149\n"
      "v_mul_f32 v150, v205, v150\n"
      "v_mul_f32 v151, v205, v151\n"
      "v_mul_f32 v152, v205, v152\n"
      "v_mul_f32 v153, v205, v153\n"
      "v_mul_f32 v154, v205, v154\n"
      "v_mul_f32 v155, v205, v155\n"
      "v_mul_f32 v156, v205, v156\n"
      "v_mul_f32 v157, v205, v157\n"
      "v_mul_f32 v158, v205, v158\n"
      "v_mul_f32 v159, v205, v159\n"
      "v_mul_f32 v160, v205, v160\n"
      "v_mul_f32 v161, v205, v161\n"
      "v_mul_f32 v162, v205, v162\n"
      "v_mul_f32 v163, v205, v163\n"
      "v_mul_f32 v164, v205, v164\n"
      "v_mul_f32 v165, v205, v165\n"
      "v_mul_f32 v166, v205, v166\n"
      "v_mul_f32 v167, v205, v167\n"
      "v_cvt_pk_bf16_f32 v200, v192, v193\n"
      "v_cvt_pk_bf16_f32 v201, v194, v195\n"
      "v_cvt_pk_bf16_f32 v202, v196, v197\n"
      "v_cvt_pk_bf16_f32 v203, v198, v199\n"
      "s_nop 1\n"
      "s_setpc_b64 s[38:39]\n"
      ".Lgq_rarep1:\n"
      "s_nop 7\n"
      "v_max3_f32 v204, v96, v97, v98\n"
      "v_max3_f32 v204, v204, v99, v100\n"
      "v_max3_f32 v204, v204, v101, v102\n"
      "v_max_f32 v204, v204, v103\n"
      "v_mov_b32 v205, v204\n"
      "s_nop 1\n"
      "v_permlane16_swap_b32 v205, v204\n"
      "v_max_f32 v204, v204, v205\n"
      "v_mov_b32 v205, v204\n"
      "s_nop 1\n"
      "v_permlane32_swap_b32 v205, v204\n"
      "v_max_f32 v204, v204, v205\n"
      "v_add_f32 v204, 0x41700000, v204\n"
      "v_max_f32 v204, 0, v204\n"
      "v_exp_f32 v205, -v204\n"
      "v_sub_f32 v24, v24, v204\n"
      "v_sub_f32 v25, v25, v204\n"
      "v_sub_f32 v26, v26, v204\n"
      "v_sub_f32 v27, v27, v204\n"
      "v_sub_f32 v96, v96, v204\n"
      "v_sub_f32 v184, v184, v204\n"
      "v_sub_f32 v97, v97, v204\n"
      "v_sub_f32 v185, v185, v204\n"
      "v_sub_f32 v98, v98, v204\n"
      "v_sub_f32 v186, v186, v204\n"
      "v_sub_f32 v99, v99, v204\n"
      "v_sub_f32 v187, v187, v204\n"
      "v_sub_f32 v100, v100, v204\n"
      "v_sub_f32 v188, v188, v204\n"
      "v_sub_f32 v101, v101, v204\n"
      "v_sub_f32 v189, v189, v204\n"
      "v_sub_f32 v102, v102, v204\n"
      "v_sub_f32 v190, v190, v204\n"
      "v_sub_f32 v103, v103, v204\n"
      "v_sub_f32 v191, v191, v204\n"
      "v_exp_f32 v192, v96\n"
      "v_exp_f32 v193, v97\n"
      "v_exp_f32 v194, v98\n"
      "v_exp_f32 v195, v99\n"
      "v_exp_f32 v196, v100\n"
      "v_exp_f32 v197, v101\n"
      "v_exp_f32 v198, v102\n"
      "v_exp_f32 v199, v103\n"
      "v_mul_f32 v132, v205, v132\n"
      "v_mul_f32 v133, v205, v133\n"
      "v_mul_f32 v134, v205, v134\n"
      "v_mul_f32 v135, v205, v135\n"
      "v_mul_f32 v136, v205, v136\n"
      "v_mul_f32 v137, v205, v137\n"
      "v_mul_f32 v138, v205, v138\n"
      "v_mul_f32 v139, v205, v139\n"
      "v_mul_f32 v140, v205, v140\n"
      "v_mul_f32 v141, v205, v141\n"
      "v_mul_f32 v142, v205, v142\n"
      "v_mul_f32 v143, v205, v143\n"
      "v_mul_f32 v144, v205, v144\n"
      "v_mul_f32 v145, v205, v145\n"
      "v_mul_f32 v146, v205, v146\n"
      "v_mul_f32 v147, v205, v147\n"
      "v_mul_f32 v148, v205, v148\n"
      "v_mul_f32 v149, v205, v149\n"
      "v_mul_f32 v150, v205, v150\n"
      "v_mul_f32 v151, v205, v151\n"
      "v_mul_f32 v152, v205, v152\n"
      "v_mul_f32 v153, v205, v153\n"
      "v_mul_f32 v154, v205, v154\n"
      "v_mul_f32 v155, v205, v155\n"
      "v_mul_f32 v156, v205, v156\n"
      "v_mul_f32 v157, v205, v157\n"
      "v_mul_f32 v158, v205, v158\n"
      "v_mul_f32 v159, v205, v159\n"
      "v_mul_f32 v160, v205, v160\n"
      "v_mul_f32 v161, v205, v161\n"
      "v_mul_f32 v162, v205, v162\n"
      "v_mul_f32 v163, v205, v163\n"
      "v_mul_f32 v164, v205, v164\n"
      "v_mul_f32 v165, v205, v165\n"
      "v_mul_f32 v166, v205, v166\n"
      "v_mul_f32 v167, v205, v167\n"
      "v_cvt_pk_bf16_f32 v200, v192, v193\n"
      "v_cvt_pk_bf16_f32 v201, v194, v195\n"
      "v_cvt_pk_bf16_f32 v202, v196, v197\n"
      "v_cvt_pk_bf16_f32 v203, v198, v199\n"
      "s_nop 1\n"
      "s_setpc_b64 s[38:39]\n"
      ".Lgq_pad3593:\n"
      "s_cbranch_execz .Lgq_pad3655\n"
      ".Lgq_f1:\n"
      "ds_read2st64_b32 v[34:35], v32 offset1:2\n"
      "ds_read2st64_b32 v[36:37], v32 offset0:4 offset1:6\n"
      "s_sub_u32 s23, s23, 1\n"
      "s_cbranch_scc1 .Lgq_end\n"
      "s_waitcnt lgkmcnt(0)\n"
      "v_or3_b32 v34, v34, v36, v37\n"
      "v_cmp_eq_u64 vcc, 0, v[34:35]\n"
      "s_bcnt1_i32_b64 s54, vcc\n"
      "s_cmp_ge_i32 s54, 1\n"
      "s_cbranch_scc1 .Lgq_fr1\n"
      "s_sleep 1\n"
      "s_branch .Lgq_f1\n"
      ".Lgq_f2:\n"
      "ds_read2st64_b32 v[34:35], v32 offset1:2\n"
      "ds_read2st64_b32 v[36:37], v32 offset0:4 offset1:6\n"
      "s_sub_u32 s23, s23, 1\n"
      "s_cbranch_scc1 .Lgq_end\n"
      "s_waitcnt lgkmcnt(0)\n"
      "v_or3_b32 v34, v34, v36, v37\n"
      "v_cmp_eq_u64 vcc, 0, v[34:35]\n"
      "s_bcnt1_i32_b64 s54, vcc\n"
      "s_cmp_ge_i32 s54, 2\n"
      "s_cbranch_scc1 .Lgq_fr2\n"
      "s_sleep 1\n"
      "s_branch .Lgq_f2\n"
      ".Lgq_f3:\n"
      "ds_read2st64_b32 v[34:35], v32 offset1:2\n"
      "ds_read2st64_b32 v[36:37], v32 offset0:4 offset1:6\n"
      "s_sub_u32 s23, s23, 1\n"
      "s_cbranch_scc1 .Lgq_end\n"
      "s_waitcnt lgkmcnt(0)\n"
      "v_or3_b32 v34, v34, v36, v37\n"
      "v_cmp_eq_u64 vcc, 0, v[34:35]\n"
      "s_bcnt1_i32_b64 s54, vcc\n"
      "s_cmp_ge_i32 s54, 3\n"
      "s_cbranch_scc1 .Lgq_fr3\n"
      "s_sleep 1\n"
      "s_branch .Lgq_f3\n"
      ".Lgq_spf:\n"
      "s_lshl2_add_u32 s26, s20, s67\n"
      "s_mul_i32 s26, s26, 18432\n"
      "s_mul_i32 s27, s66, 589824\n"
      "s_add_u32 s26, s26, s27\n"
      "s_add_u32 s60, s12, s26\n"
      "s_addc_u32 s61, s13, 0\n"
      "s_add_u32 s26, s52, 50688\n"
      ".Lgq_spf1:\n"
      "s_load_dword s65, s[60:61], s52 offset:0\n"
      "s_load_dword s65, s[60:61], s52 offset:64\n"
      "s_load_dword s65, s[60:61], s52 offset:128\n"
      "s_load_dword s65, s[60:61], s52 offset:192\n"
      "s_load_dword s65, s[60:61], s52 offset:256\n"
      "s_load_dword s65, s[60:61], s52 offset:320\n"
      "s_load_dword s65, s[60:61], s52 offset:384\n"
      "s_load_dword s65, s[60:61], s52 offset:448\n"
      "s_add_u32 s52, s52, 512\n"
      "s_cmp_lt_u32 s52, s53\n"
      "s_cbranch_scc1 .Lgq_spf1\n"
      "s_endpgm\n"
      ".Lgq_pad3655:\n"
      "s_cbranch_execz .Lgq_pad3728\n"
      ".Lgq_extra:\n"
      "s_cmp_eq_u32 s22, 6\n"
      "s_cselect_b32 s52, 0, 9216\n"
      "s_mov_b32 s53, 18432\n"
      "s_cselect_b32 s53, 9216, s53\n"
      "s_mov_b32 s26, 155704\n"
      "s_cselect_b32 s26, 155704, s26\n"
      "v_mov_b32 v2, s26\n"
      ".Lgq_extra1:\n"
      "s_sleep 2\n"
      "ds_read_b32 v3, v2\n"
      "s_sub_u32 s23, s23, 1\n"
      "s_cbranch_scc1 .Lgq_end\n"
      "s_waitcnt lgkmcnt(0)\n"
      "v_cmp_ne_u32 vcc, 0, v3\n"
      "s_cbranch_vccnz .Lgq_extra1\n"
      "s_branch .Lgq_spf\n"
      ".Lgq_kmin1:\n"
      "s_sleep 1\n"
      "s_add_u32 s28, s28, 4\n"
      "s_branch .Lgq_kmin\n"
      ".Lgq_f4:\n"
      "ds_read2st64_b32 v[34:35], v32 offset1:2\n"
      "ds_read2st64_b32 v[36:37], v32 offset0:4 offset1:6\n"
      "s_sub_u32 s23, s23, 1\n"
      "s_cbranch_scc1 .Lgq_end\n"
      "s_waitcnt lgkmcnt(0)\n"
      "v_or3_b32 v34, v34, v36, v37\n"
      "v_cmp_eq_u64 vcc, 0, v[34:35]\n"
      "s_bcnt1_i32_b64 s54, vcc\n"
      "s_cmp_ge_i32 s54, 4\n"
      "s_cbranch_scc1 .Lgq_fr4\n"
      "s_sleep 1\n"
      "s_branch .Lgq_f4\n"
      ".Lgq_f5:\n"
      "ds_read2st64_b32 v[34:35], v32 offset1:2\n"
      "ds_read2st64_b32 v[36:37], v32 offset0:4 offset1:6\n"
      "s_sub_u32 s23, s23, 1\n"
      "s_cbranch_scc1 .Lgq_end\n"
      "s_waitcnt lgkmcnt(0)\n"
      "v_or3_b32 v34, v34, v36, v37\n"
      "v_cmp_eq_u64 vcc, 0, v[34:35]\n"
      "s_bcnt1_i32_b64 s54, vcc\n"
      "s_cmp_ge_i32 s54, s19\n"
      "s_cbranch_scc1 .Lgq_fr5\n"
      "s_sleep 1\n"
      "s_branch .Lgq_f5\n"
      ".Lgq_f6:\n"
      "ds_read2st64_b32 v[34:35], v32 offset1:2\n"
      "ds_read2st64_b32 v[36:37], v32 offset0:4 offset1:6\n"
      "s_sub_u32 s23, s23, 1\n"
      "s_cbranch_scc1 .Lgq_end\n"
      "s_waitcnt lgkmcnt(0)\n"
      "v_or3_b32 v34, v34, v36, v37\n"
      "v_cmp_eq_u64 vcc, 0, v[34:35]\n"
      "s_bcnt1_i32_b64 s54, vcc\n"
      "s_cmp_ge_i32 s54, 13\n"
      "s_cbranch_scc1 .Lgq_fr6\n"
      "s_sleep 1\n"
      "s_branch .Lgq_f6\n"
      ".Lgq_f7:\n"
      "ds_read2st64_b32 v[34:35], v32 offset1:2\n"
      "ds_read2st64_b32 v[36:37], v32 offset0:4 offset1:6\n"
      "s_sub_u32 s23, s23, 1\n"
      "s_cbranch_scc1 .Lgq_end\n"
      "s_waitcnt lgkmcnt(0)\n"
      "v_or3_b32 v34, v34, v36, v37\n"
      "v_cmp_eq_u64 vcc, 0, v[34:35]\n"
      "s_bcnt1_i32_b64 s54, vcc\n"
      "s_cmp_ge_i32 s54, s19\n"
      "s_cbranch_scc1 .Lgq_fr7\n"
      "s_sleep 1\n"
      ".Lgq_pad3728:\n"
      "s_cbranch_execz .Lgq_pad3792\n"
      "s_branch .Lgq_f7\n"
      ".Lgq_f8:\n"
      "ds_read2st64_b32 v[34:35], v32 offset1:2\n"
      "ds_read2st64_b32 v[36:37], v32 offset0:4 offset1:6\n"
      "s_sub_u32 s23, s23, 1\n"
      "s_cbranch_scc1 .Lgq_end\n"
      "s_waitcnt lgkmcnt(0)\n"
      "v_or3_b32 v34, v34, v36, v37\n"
      "v_cmp_eq_u64 vcc, 0, v[34:35]\n"
      "s_bcnt1_i32_b64 s54, vcc\n"
      "s_cmp_ge_i32 s54, s19\n"
      "s_cbranch_scc1 .Lgq_fr8\n"
      "s_sleep 1\n"
      "s_branch .Lgq_f8\n"
      ".Lgq_f9:\n"
      "ds_read2st64_b32 v[34:35], v32 offset1:2\n"
      "ds_read2st64_b32 v[36:37], v32 offset0:4 offset1:6\n"
      "s_sub_u32 s23, s23, 1\n"
      "s_cbranch_scc1 .Lgq_end\n"
      "s_waitcnt lgkmcnt(0)\n"
      "v_or3_b32 v34, v34, v36, v37\n"
      "v_cmp_eq_u64 vcc, 0, v[34:35]\n"
      "s_bcnt1_i32_b64 s54, vcc\n"
      "s_cmp_ge_i32 s54, 16\n"
      "s_cbranch_scc1 .Lgq_fr9\n"
      "s_sleep 1\n"
      "s_branch .Lgq_f9\n"
      ".Lgq_w11:\n"
      "ds_read_b64 v[14:15], v10\n"
      "s_sub_u32 s23, s23, 1\n"
      "s_cbranch_scc1 .Lgq_end\n"
      "s_waitcnt lgkmcnt(0)\n"
      "v_cmp_eq_u64 s[36:37], 0, v[14:15]\n"
      "s_bitcmp1_b64 s[36:37], 1\n"
      "s_cbranch_scc1 .Lgq_r11\n"
      "s_branch .Lgq_w11\n"
      ".Lgq_w12:\n"
      "ds_read_b64 v[14:15], v10\n"
      "s_sub_u32 s23, s23, 1\n"
      "s_cbranch_scc1 .Lgq_end\n"
      "s_waitcnt lgkmcnt(0)\n"
      "v_cmp_eq_u64 s[36:37], 0, v[14:15]\n"
      "s_bitcmp1_b64 s[36:37], 2\n"
      "s_cbranch_scc1 .Lgq_r12\n"
      "s_branch .Lgq_w12\n"
      ".Lgq_w13:\n"
      "ds_read_b64 v[14:15], v10\n"
      "s_sub_u32 s23, s23, 1\n"
      "s_cbranch_scc1 .Lgq_end\n"
      "s_waitcnt lgkmcnt(0)\n"
      "v_cmp_eq_u64 s[36:37], 0, v[14:15]\n"
      "s_bitcmp1_b64 s[36:37], 3\n"
      "s_cbranch_scc1 .Lgq_r13\n"
      "s_branch .Lgq_w13\n"
      ".Lgq_w14:\n"
      "ds_read_b64 v[14:15], v10\n"
      "s_sub_u32 s23, s23, 1\n"
      "s_cbranch_scc1 .Lgq_end\n"
      "s_waitcnt lgkmcnt(0)\n"
      "v_cmp_eq_u64 s[36:37], 0, v[14:15]\n"
      "s_bitcmp1_b64 s[36:37], 4\n"
      "s_cbranch_scc1 .Lgq_r14\n"
      "s_branch .Lgq_w14\n"
      ".Lgq_pad3792:\n"
      "s_cbranch_execz .Lgq_pad3865\n"
      ".Lgq_w15:\n"
      "ds_read_b64 v[14:15], v10\n"
      "s_sub_u32 s23, s23, 1\n"
      "s_cbranch_scc1 .Lgq_end\n"
      "s_waitcnt lgkmcnt(0)\n"
      "v_cmp_eq_u64 s[36:37], 0, v[14:15]\n"
      "s_bitcmp1_b64 s[36:37], 5\n"
      "s_cbranch_scc1 .Lgq_r15\n"
      "s_branch .Lgq_w15\n"
      ".Lgq_w16:\n"
      "ds_read_b64 v[14:15], v10\n"
      "s_sub_u32 s23, s23, 1\n"
      "s_cbranch_scc1 .Lgq_end\n"
      "s_waitcnt lgkmcnt(0)\n"
      "v_cmp_eq_u64 s[36:37], 0, v[14:15]\n"
      "s_bitcmp1_b64 s[36:37], 6\n"
      "s_cbranch_scc1 .Lgq_r16\n"
      "s_branch .Lgq_w16\n"
      ".Lgq_w17:\n"
      "ds_read_b64 v[14:15], v10\n"
      "s_sub_u32 s23, s23, 1\n"
      "s_cbranch_scc1 .Lgq_end\n"
      "s_waitcnt lgkmcnt(0)\n"
      "v_cmp_eq_u64 s[36:37], 0, v[14:15]\n"
      "s_bitcmp1_b64 s[36:37], 7\n"
      "s_cbranch_scc1 .Lgq_r17\n"
      "s_branch .Lgq_w17\n"
      ".Lgq_w18:\n"
      "ds_read_b64 v[14:15], v10\n"
      "s_sub_u32 s23, s23, 1\n"
      "s_cbranch_scc1 .Lgq_end\n"
      "s_waitcnt lgkmcnt(0)\n"
      "v_cmp_eq_u64 s[36:37], 0, v[14:15]\n"
      "s_bitcmp1_b64 s[36:37], 8\n"
      "s_cbranch_scc1 .Lgq_r18\n"
      "s_branch .Lgq_w18\n"
      ".Lgq_w19:\n"
      "ds_read_b64 v[14:15], v10\n"
      "s_sub_u32 s23, s23, 1\n"
      "s_cbranch_scc1 .Lgq_end\n"
      "s_waitcnt lgkmcnt(0)\n"
      "v_cmp_eq_u64 s[36:37], 0, v[14:15]\n"
      "s_bitcmp1_b64 s[36:37], 9\n"
      "s_cbranch_scc1 .Lgq_r19\n"
      "s_branch .Lgq_w19\n"
      ".Lgq_w20:\n"
      "ds_read_b64 v[14:15], v10\n"
      "s_sub_u32 s23, s23, 1\n"
      "s_cbranch_scc1 .Lgq_end\n"
      "s_waitcnt lgkmcnt(0)\n"
      "v_cmp_eq_u64 s[36:37], 0, v[14:15]\n"
      "s_bitcmp1_b64 s[36:37], 10\n"
      "s_cbranch_scc1 .Lgq_r20\n"
      "s_branch .Lgq_w20\n"
      ".Lgq_w21:\n"
      "ds_read_b64 v[14:15], v10\n"
      "s_sub_u32 s23, s23, 1\n"
      "s_cbranch_scc1 .Lgq_end\n"
      "s_waitcnt lgkmcnt(0)\n"
      "v_cmp_eq_u64 s[36:37], 0, v[14:15]\n"
      "s_bitcmp1_b64 s[36:37], 11\n"
      "s_cbranch_scc1 .Lgq_r21\n"
      "s_branch .Lgq_w21\n"
      ".Lgq_w22:\n"
      "ds_read_b64 v[14:15], v10\n"
      "s_sub_u32 s23, s23, 1\n"
      "s_cbranch_scc1 .Lgq_end\n"
      "s_waitcnt lgkmcnt(0)\n"
      "v_cmp_eq_u64 s[36:37], 0, v[14:15]\n"
      "s_bitcmp1_b64 s[36:37], 12\n"
      "s_cbranch_scc1 .Lgq_r22\n"
      "s_branch .Lgq_w22\n"
      ".Lgq_pad3865:\n"
      "s_cbranch_execz .Lgq_pad3911\n"
      ".Lgq_w23:\n"
      "ds_read_b64 v[14:15], v10\n"
      "s_sub_u32 s23, s23, 1\n"
      "s_cbranch_scc1 .Lgq_end\n"
      "s_waitcnt lgkmcnt(0)\n"
      "v_cmp_eq_u64 s[36:37], 0, v[14:15]\n"
      "s_bitcmp1_b64 s[36:37], 13\n"
      "s_cbranch_scc1 .Lgq_r23\n"
      "s_branch .Lgq_w23\n"
      ".Lgq_w24:\n"
      "ds_read_b64 v[14:15], v10\n"
      "s_sub_u32 s23, s23, 1\n"
      "s_cbranch_scc1 .Lgq_end\n"
      "s_waitcnt lgkmcnt(0)\n"
      "v_cmp_eq_u64 s[36:37], 0, v[14:15]\n"
      "s_bitcmp1_b64 s[36:37], 14\n"
      "s_cbranch_scc1 .Lgq_r24\n"
      "s_branch .Lgq_w24\n"
      ".Lgq_w25:\n"
      "ds_read_b64 v[14:15], v10\n"
      "s_sub_u32 s23, s23, 1\n"
      "s_cbranch_scc1 .Lgq_end\n"
      "s_waitcnt lgkmcnt(0)\n"
      "v_cmp_eq_u64 s[36:37], 0, v[14:15]\n"
      "s_bitcmp1_b64 s[36:37], 15\n"
      "s_cbranch_scc1 .Lgq_r25\n"
      "s_branch .Lgq_w25\n"
      ".Lgq_w26:\n"
      "ds_read_b64 v[14:15], v10\n"
      "s_sub_u32 s23, s23, 1\n"
      "s_cbranch_scc1 .Lgq_end\n"
      "s_waitcnt lgkmcnt(0)\n"
      "v_cmp_eq_u64 s[36:37], 0, v[14:15]\n"
      "s_bitcmp1_b64 s[36:37], 16\n"
      "s_cbranch_scc1 .Lgq_r26\n"
      "s_branch .Lgq_w26\n"
      ".Lgq_w27:\n"
      "ds_read_b64 v[14:15], v10\n"
      "s_sub_u32 s23, s23, 1\n"
      "s_cbranch_scc1 .Lgq_end\n"
      "s_waitcnt lgkmcnt(0)\n"
      "v_cmp_eq_u64 s[36:37], 0, v[14:15]\n"
      "s_bitcmp1_b64 s[36:37], 17\n"
      "s_cbranch_scc1 .Lgq_r27\n"
      "s_branch .Lgq_w27\n"
      ".Lgq_pad3911:\n"
      "s_cbranch_execz .Lgq_pad3984\n"
      ".Lgq_w28:\n"
      "ds_read_b64 v[14:15], v10\n"
      "s_sub_u32 s23, s23, 1\n"
      "s_cbranch_scc1 .Lgq_end\n"
      "s_waitcnt lgkmcnt(0)\n"
      "v_cmp_eq_u64 s[36:37], 0, v[14:15]\n"
      "s_bitcmp1_b64 s[36:37], 18\n"
      "s_cbranch_scc1 .Lgq_r28\n"
      "s_branch .Lgq_w28\n"
      ".Lgq_w29:\n"
      "ds_read_b64 v[14:15], v10\n"
      "s_sub_u32 s23, s23, 1\n"
      "s_cbranch_scc1 .Lgq_end\n"
      "s_waitcnt lgkmcnt(0)\n"
      "v_cmp_eq_u64 s[36:37], 0, v[14:15]\n"
      "s_bitcmp1_b64 s[36:37], 19\n"
      "s_cbranch_scc1 .Lgq_r29\n"
      "s_branch .Lgq_w29\n"
      ".Lgq_w30:\n"
      "ds_read_b64 v[14:15], v10\n"
      "s_sub_u32 s23, s23, 1\n"
      "s_cbranch_scc1 .Lgq_end\n"
      "s_waitcnt lgkmcnt(0)\n"
      "v_cmp_eq_u64 s[36:37], 0, v[14:15]\n"
      "s_bitcmp1_b64 s[36:37], 20\n"
      "s_cbranch_scc1 .Lgq_r30\n"
      "s_branch .Lgq_w30\n"
      ".Lgq_w31:\n"
      "ds_read_b64 v[14:15], v10\n"
      "s_sub_u32 s23, s23, 1\n"
      "s_cbranch_scc1 .Lgq_end\n"
      "s_waitcnt lgkmcnt(0)\n"
      "v_cmp_eq_u64 s[36:37], 0, v[14:15]\n"
      "s_bitcmp1_b64 s[36:37], 21\n"
      "s_cbranch_scc1 .Lgq_r31\n"
      "s_branch .Lgq_w31\n"
      ".Lgq_w32:\n"
      "ds_read_b64 v[14:15], v10\n"
      "s_sub_u32 s23, s23, 1\n"
      "s_cbranch_scc1 .Lgq_end\n"
      "s_waitcnt lgkmcnt(0)\n"
      "v_cmp_eq_u64 s[36:37], 0, v[14:15]\n"
      "s_bitcmp1_b64 s[36:37], 22\n"
      "s_cbranch_scc1 .Lgq_r32\n"
      "s_branch .Lgq_w32\n"
      ".Lgq_w33:\n"
      "ds_read_b64 v[14:15], v10\n"
      "s_sub_u32 s23, s23, 1\n"
      "s_cbranch_scc1 .Lgq_end\n"
      "s_waitcnt lgkmcnt(0)\n"
      "v_cmp_eq_u64 s[36:37], 0, v[14:15]\n"
      "s_bitcmp1_b64 s[36:37], 23\n"
      "s_cbranch_scc1 .Lgq_r33\n"
      "s_branch .Lgq_w33\n"
      ".Lgq_w34:\n"
      "ds_read_b64 v[14:15], v10\n"
      "s_sub_u32 s23, s23, 1\n"
      "s_cbranch_scc1 .Lgq_end\n"
      "s_waitcnt lgkmcnt(0)\n"
      "v_cmp_eq_u64 s[36:37], 0, v[14:15]\n"
      "s_bitcmp1_b64 s[36:37], 24\n"
      "s_cbranch_scc1 .Lgq_r34\n"
      "s_branch .Lgq_w34\n"
      ".Lgq_w35:\n"
      "ds_read_b64 v[14:15], v10\n"
      "s_sub_u32 s23, s23, 1\n"
      "s_cbranch_scc1 .Lgq_end\n"
      "s_waitcnt lgkmcnt(0)\n"
      "v_cmp_eq_u64 s[36:37], 0, v[14:15]\n"
      "s_bitcmp1_b64 s[36:37], 25\n"
      "s_cbranch_scc1 .Lgq_r35\n"
      "s_branch .Lgq_w35\n"
      ".Lgq_pad3984:\n"
      "s_cbranch_execz .Lgq_pad4039\n"
      ".Lgq_w36:\n"
      "ds_read_b64 v[14:15], v10\n"
      "s_sub_u32 s23, s23, 1\n"
      "s_cbranch_scc1 .Lgq_end\n"
      "s_waitcnt lgkmcnt(0)\n"
      "v_cmp_eq_u64 s[36:37], 0, v[14:15]\n"
      "s_bitcmp1_b64 s[36:37], 26\n"
      "s_cbranch_scc1 .Lgq_r36\n"
      "s_branch .Lgq_w36\n"
      ".Lgq_w37:\n"
      "ds_read_b64 v[14:15], v10\n"
      "s_sub_u32 s23, s23, 1\n"
      "s_cbranch_scc1 .Lgq_end\n"
      "s_waitcnt lgkmcnt(0)\n"
      "v_cmp_eq_u64 s[36:37], 0, v[14:15]\n"
      "s_bitcmp1_b64 s[36:37], 27\n"
      "s_cbranch_scc1 .Lgq_r37\n"
      "s_branch .Lgq_w37\n"
      ".Lgq_w38:\n"
      "ds_read_b64 v[14:15], v10\n"
      "s_sub_u32 s23, s23, 1\n"
      "s_cbranch_scc1 .Lgq_end\n"
      "s_waitcnt lgkmcnt(0)\n"
      "v_cmp_eq_u64 s[36:37], 0, v[14:15]\n"
      "s_bitcmp1_b64 s[36:37], 29\n"
      "s_cbranch_scc1 .Lgq_r38\n"
      "s_branch .Lgq_w38\n"
      ".Lgq_w39:\n"
      "ds_read_b64 v[14:15], v10\n"
      "s_sub_u32 s23, s23, 1\n"
      "s_cbranch_scc1 .Lgq_end\n"
      "s_waitcnt lgkmcnt(0)\n"
      "v_cmp_eq_u64 s[36:37], 0, v[14:15]\n"
      "s_bitcmp1_b64 s[36:37], 31\n"
      "s_cbranch_scc1 .Lgq_r39\n"
      "s_branch .Lgq_w39\n"
      ".Lgq_w40:\n"
      "ds_read_b64 v[14:15], v10\n"
      "s_sub_u32 s23, s23, 1\n"
      "s_cbranch_scc1 .Lgq_end\n"
      "s_waitcnt lgkmcnt(0)\n"
      "v_cmp_eq_u64 s[36:37], 0, v[14:15]\n"
      "s_bitcmp1_b64 s[36:37], 33\n"
      "s_cbranch_scc1 .Lgq_r40\n"
      "s_branch .Lgq_w40\n"
      ".Lgq_w41:\n"
      "ds_read_b64 v[14:15], v10\n"
      "s_sub_u32 s23, s23, 1\n"
      "s_cbranch_scc1 .Lgq_end\n"
      "s_waitcnt lgkmcnt(0)\n"
      "v_cmp_eq_u64 s[36:37], 0, v[14:15]\n"
      "s_bitcmp1_b64 s[36:37], 35\n"
      "s_cbranch_scc1 .Lgq_r41\n"
      "s_branch .Lgq_w41\n"
      ".Lgq_pad4039:\n"
      "s_cbranch_execz .Lgq_pad4097\n"
      ".Lgq_reh1:\n"
      "v_add_u32 v184, s24, v184\n"
      "v_add_u32 v188, s24, v188\n"
      "v_add_u32 v192, s24, v192\n"
      "v_add_u32 v196, s24, v196\n"
      "s_mov_b64 exec, vcc\n"
      "buffer_load_dwordx4 v[184:187], v4, s[28:31], s45 offen offset:0 sc1\n"
      "buffer_load_dwordx4 v[188:191], v4, s[28:31], s45 offen offset:1024 sc1\n"
      "buffer_load_dwordx4 v[192:195], v4, s[28:31], s45 offen offset:2048 sc1\n"
      "buffer_load_dwordx4 v[196:199], v4, s[28:31], s45 offen offset:3072 sc1\n"
      "buffer_load_dwordx2 v[236:237], v5, s[28:31], s45 offen sc1\n"
      "s_sub_u32 s23, s23, 16\n"
      "s_cbranch_scc1 .Lgq_end\n"
      "s_mov_b64 exec, -1\n"
      "s_waitcnt vmcnt(0)\n"
      "v_add3_u32 v6, s24, v184, v186\n"
      "v_add3_u32 v7, s25, v185, v187\n"
      "v_subrev_u32 v184, s24, v184\n"
      "v_add3_u32 v6, v6, v188, v190\n"
      "v_add3_u32 v7, v7, v189, v191\n"
      "v_subrev_u32 v188, s24, v188\n"
      "v_add3_u32 v6, v6, v192, v194\n"
      "v_add3_u32 v7, v7, v193, v195\n"
      "v_subrev_u32 v192, s24, v192\n"
      "v_add3_u32 v6, v6, v196, v198\n"
      "v_add3_u32 v7, v7, v197, v199\n"
      "v_subrev_u32 v196, s24, v196\n"
      "v_cmp_ne_u64 vcc, v[6:7], v[236:237]\n"
      "s_cbranch_vccnz .Lgq_reh1\n"
      "s_branch .Lgq_okh1\n"
      ".Lgq_w42:\n"
      "ds_read_b64 v[14:15], v10\n"
      "s_sub_u32 s23, s23, 1\n"
      "s_cbranch_scc1 .Lgq_end\n"
      "s_waitcnt lgkmcnt(0)\n"
      "v_cmp_eq_u64 s[36:37], 0, v[14:15]\n"
      "s_bitcmp1_b64 s[36:37], 37\n"
      "s_cbranch_scc1 .Lgq_r42\n"
      "s_branch .Lgq_w42\n"
      ".Lgq_w43:\n"
      "ds_read_b64 v[14:15], v10\n"
      "s_sub_u32 s23, s23, 1\n"
      "s_cbranch_scc1 .Lgq_end\n"
      "s_waitcnt lgkmcnt(0)\n"
      "v_cmp_eq_u64 s[36:37], 0, v[14:15]\n"
      "s_bitcmp1_b64 s[36:37], 39\n"
      "s_cbranch_scc1 .Lgq_r43\n"
      "s_branch .Lgq_w43\n"
      ".Lgq_w44:\n"
      "ds_read_b64 v[14:15], v10\n"
      "s_sub_u32 s23, s23, 1\n"
      "s_cbranch_scc1 .Lgq_end\n"
      "s_waitcnt lgkmcnt(0)\n"
      "v_cmp_eq_u64 s[36:37], 0, v[14:15]\n"
      "s_bitcmp1_b64 s[36:37], 41\n"
      "s_cbranch_scc1 .Lgq_r44\n"
      "s_branch .Lgq_w44\n"
      ".Lgq_pad4097:\n"
      "s_cbranch_execz .Lgq_pad4137\n"
      ".Lgq_reh2:\n"
      "v_add_u32 v200, s24, v200\n"
      "v_add_u32 v204, s24, v204\n"
      "v_add_u32 v208, s24, v208\n"
      "v_add_u32 v212, s24, v212\n"
      "s_mov_b64 exec, vcc\n"
      "buffer_load_dwordx4 v[200:203], v4, s[28:31], s46 offen offset:0 sc1\n"
      "buffer_load_dwordx4 v[204:207], v4, s[28:31], s46 offen offset:1024 sc1\n"
      "buffer_load_dwordx4 v[208:211], v4, s[28:31], s46 offen offset:2048 sc1\n"
      "buffer_load_dwordx4 v[212:215], v4, s[28:31], s46 offen offset:3072 sc1\n"
      "buffer_load_dwordx2 v[238:239], v5, s[28:31], s46 offen sc1\n"
      "s_sub_u32 s23, s23, 16\n"
      "s_cbranch_scc1 .Lgq_end\n"
      "s_mov_b64 exec, -1\n"
      "s_waitcnt vmcnt(0)\n"
      "v_add3_u32 v6, s24, v200, v202\n"
      "v_add3_u32 v7, s25, v201, v203\n"
      "v_subrev_u32 v200, s24, v200\n"
      "v_add3_u32 v6, v6, v204, v206\n"
      "v_add3_u32 v7, v7, v205, v207\n"
      "v_subrev_u32 v204, s24, v204\n"
      "v_add3_u32 v6, v6, v208, v210\n"
      "v_add3_u32 v7, v7, v209, v211\n"
      "v_subrev_u32 v208, s24, v208\n"
      "v_add3_u32 v6, v6, v212, v214\n"
      "v_add3_u32 v7, v7, v213, v215\n"
      "v_subrev_u32 v212, s24, v212\n"
      "v_cmp_ne_u64 vcc, v[6:7], v[238:239]\n"
      "s_cbranch_vccnz .Lgq_reh2\n"
      "s_branch .Lgq_okh2\n"
      ".Lgq_w45:\n"
      "ds_read_b64 v[14:15], v10\n"
      "s_sub_u32 s23, s23, 1\n"
      "s_cbranch_scc1 .Lgq_end\n"
      "s_waitcnt lgkmcnt(0)\n"
      "v_cmp_eq_u64 s[36:37], 0, v[14:15]\n"
      "s_bitcmp1_b64 s[36:37], 43\n"
      "s_cbranch_scc1 .Lgq_r45\n"
      "s_branch .Lgq_w45\n"
      ".Lgq_pad4137:\n"
      "s_cbranch_execz .Lgq_pad4223\n"
      ".Lgq_reh3:\n"
      "v_add_u32 v216, s24, v216\n"
      "v_add_u32 v220, s24, v220\n"
      "v_add_u32 v224, s24, v224\n"
      "v_add_u32 v228, s24, v228\n"
      "s_mov_b64 exec, vcc\n"
      "buffer_load_dwordx4 v[216:219], v4, s[28:31], s47 offen offset:0 sc1\n"
      "buffer_load_dwordx4 v[220:223], v4, s[28:31], s47 offen offset:1024 sc1\n"
      "buffer_load_dwordx4 v[224:227], v4, s[28:31], s47 offen offset:2048 sc1\n"
      "buffer_load_dwordx4 v[228:231], v4, s[28:31], s47 offen offset:3072 sc1\n"
      "buffer_load_dwordx2 v[240:241], v5, s[28:31], s47 offen sc1\n"
      "s_sub_u32 s23, s23, 16\n"
      "s_cbranch_scc1 .Lgq_end\n"
      "s_mov_b64 exec, -1\n"
      "s_waitcnt vmcnt(0)\n"
      "v_add3_u32 v6, s24, v216, v218\n"
      "v_add3_u32 v7, s25, v217, v219\n"
      "v_subrev_u32 v216, s24, v216\n"
      "v_add3_u32 v6, v6, v220, v222\n"
      "v_add3_u32 v7, v7, v221, v223\n"
      "v_subrev_u32 v220, s24, v220\n"
      "v_add3_u32 v6, v6, v224, v226\n"
      "v_add3_u32 v7, v7, v225, v227\n"
      "v_subrev_u32 v224, s24, v224\n"
      "v_add3_u32 v6, v6, v228, v230\n"
      "v_add3_u32 v7, v7, v229, v231\n"
      "v_subrev_u32 v228, s24, v228\n"
      "v_cmp_ne_u64 vcc, v[6:7], v[240:241]\n"
      "s_cbranch_vccnz .Lgq_reh3\n"
      "s_branch .Lgq_okh3\n"
      ".Lgq_rep:\n"
      "v_add_u32 v16, s24, v16\n"
      "v_add_u32 v20, s24, v20\n"
      "v_add_u32 v24, s24, v24\n"
      "v_add_u32 v28, s24, v28\n"
      "v_add_u32 v32, s24, v32\n"
      "s_mov_b64 exec, vcc\n"
      "buffer_load_dwordx4 v[16:19], v48, s[28:31], s49 offen offset:0 sc1\n"
      "buffer_load_dwordx4 v[20:23], v48, s[28:31], s49 offen offset:512 sc1\n"
      "buffer_load_dwordx4 v[24:27], v48, s[28:31], s49 offen offset:1024 sc1\n"
      "buffer_load_dwordx4 v[28:31], v48, s[28:31], s49 offen offset:1536 sc1\n"
      "buffer_load_dwordx4 v[32:35], v48, s[28:31], s49 offen offset:2048 sc1\n"
      "s_sub_u32 s23, s23, 16\n"
      "s_cbranch_scc1 .Lgq_end\n"
      "s_mov_b64 exec, -1\n"
      "s_branch .Lgq_fin\n"
      ".Lgq_rare1:\n"
      "s_call_b64 s[38:39], .Lgq_rarep1\n"
      "s_branch .Lgq_rback1\n"
      ".Lgq_rare2:\n"
      "s_call_b64 s[38:39], .Lgq_rarep0\n"
      "s_branch .Lgq_rback2\n"
      ".Lgq_rare3:\n"
      "s_call_b64 s[38:39], .Lgq_rarep1\n"
      "s_branch .Lgq_rback3\n"
      ".Lgq_rare4:\n"
      "s_call_b64 s[38:39], .Lgq_rarep0\n"
      "s_branch .Lgq_rback4\n"
      ".Lgq_rare5:\n"
      "s_call_b64 s[38:39], .Lgq_rarep1\n"
      "s_branch .Lgq_rback5\n"
      ".Lgq_rare6:\n"
      "s_call_b64 s[38:39], .Lgq_rarep0\n"
      "s_branch .Lgq_rback6\n"
      ".Lgq_rare7:\n"
      "s_call_b64 s[38:39], .Lgq_rarep1\n"
      "s_branch .Lgq_rback7\n"
      ".Lgq_rare8:\n"
      "s_call_b64 s[38:39], .Lgq_rarep0\n"
      "s_branch .Lgq_rback8\n"
      ".Lgq_rare9:\n"
      "s_call_b64 s[38:39], .Lgq_rarep1\n"
      "s_branch .Lgq_rback9\n"
      ".Lgq_rare10:\n"
      "s_call_b64 s[38:39], .Lgq_rarep0\n"
      "s_branch .Lgq_rback10\n"
      ".Lgq_rare11:\n"
      "s_call_b64 s[38:39], .Lgq_rarep1\n"
      "s_branch .Lgq_rback11\n"
      ".Lgq_rare12:\n"
      "s_call_b64 s[38:39], .Lgq_rarep0\n"
      "s_branch .Lgq_rback12\n"
      ".Lgq_rare13:\n"
      "s_call_b64 s[38:39], .Lgq_rarep1\n"
      "s_branch .Lgq_rback13\n"
      ".Lgq_pad4223:\n"
      "s_cbranch_execz .Lgq_pad573\n"
      ".Lgq_rare14:\n"
      "s_call_b64 s[38:39], .Lgq_rarep0\n"
      "s_branch .Lgq_rback14\n"
      ".Lgq_rare15:\n"
      "s_call_b64 s[38:39], .Lgq_rarep1\n"
      "s_branch .Lgq_rback15\n"
      ".Lgq_code_end:\n"
      :: "s"(__builtin_amdgcn_kernarg_segment_ptr()), "s"(blockIdx.x), "v"(threadIdx.x)
      : "v1", "v2", "v3", "v4", "v5", "v6", "v7", "v8", "v9", "v10", "v11", "v12", "v13", "v14", "v15", "v16",
        "v17", "v18", "v19", "v20", "v21", "v22", "v23", "v24", "v25", "v26", "v27", "v28", "v29", "v30", "v31", "v32",
        "v33", "v34", "v35", "v36", "v37", "v38", "v39", "v40", "v41", "v42", "v43", "v44", "v45", "v46", "v47", "v48",
        "v49", "v50", "v51", "v52", "v53", "v54", "v55", "v56", "v57", "v58", "v59", "v60", "v61", "v62", "v63", "v64",
        "v65", "v66", "v67", "v68", "v69", "v70", "v71", "v72", "v73", "v74", "v75", "v76", "v77", "v78", "v79", "v80",
        "v81", "v82", "v83", "v84", "v85", "v86", "v87", "v88", "v89", "v90", "v91", "v92", "v93", "v94", "v95", "v96",
        "v97", "v98", "v99", "v100", "v101", "v102", "v103", "v104", "v105", "v106", "v107", "v108", "v109", "v110", "v111", "v112",
        "v113", "v114", "v115", "v116", "v117", "v118", "v119", "v120", "v121", "v122", "v123", "v124", "v125", "v126", "v127", "v128",
        "v129", "v130", "v131", "v132", "v133", "v134", "v135", "v136", "v137", "v138", "v139", "v140", "v141", "v142", "v143", "v144",
        "v145", "v146", "v147", "v148", "v149", "v150", "v151", "v152", "v153", "v154", "v155", "v156", "v157", "v158", "v159", "v160",
        "v161", "v162", "v163", "v164", "v165", "v166", "v167", "v168", "v169", "v170", "v171", "v172", "v173", "v174", "v175", "v176",
        "v177", "v178", "v179", "v180", "v181", "v182", "v183", "v184", "v185", "v186", "v187", "v188", "v189", "v190", "v191", "v192",
        "v193", "v194", "v195", "v196", "v197", "v198", "v199", "v200", "v201", "v202", "v203", "v204", "v205", "v206", "v207", "v208",
        "v209", "v210", "v211", "v212", "v213", "v214", "v215", "v216", "v217", "v218", "v219", "v220", "v221", "v222", "v223", "v224",
        "v225", "v226", "v227", "v228", "v229", "v230", "v231", "v232", "v233", "v234", "v235", "v236", "v237", "v238", "v239", "v240",
        "v241", "v242", "v243", "v244", "v245", "v246", "v247", "v248", "v249", "v250", "v251", "v252", "v253", "v254", "v255", "s4",
        "s5", "s6", "s7", "s8", "s9", "s10", "s11", "s12", "s13", "s14", "s15", "s16", "s17", "s18", "s19", "s20",
        "s21", "s22", "s23", "s24", "s25", "s26", "s27", "s28", "s29", "s30", "s31", "s33", "s34", "s35", "s36", "s37",
        "s38", "s39", "s40", "s41", "s42", "s43", "s44", "s45", "s46", "s47", "s48", "s49", "s50", "s51", "s52", "s53",
        "s54", "s55", "s56", "s57", "s58", "s59", "s60", "s61", "s62", "s63", "s64", "s65", "s66", "s67", "vcc", "memory");
}

extern "C" void kernel_launch(void* const* d_in, const int* in_sizes, int n_in, void* d_out, int out_size, void* d_ws,
                              size_t ws_size, hipStream_t stream) {
  (void)hipFuncSetAttribute((const void*)k_fused, hipFuncAttributeMaxDynamicSharedMemorySize, LDS_BYTES);
  k_fused<<<256, 512, LDS_BYTES, stream>>>(d_in[0], d_in[1], d_in[2], d_in[3], d_in[4], d_out, d_ws, d_ws);
}
```
